```python
import torch

def _softmax(x, axis=-1):
    return torch.softmax(x, dim=axis)
def _gelu(x):
    return torch.nn.functional.gelu(x, approximate="tanh")
def _silu(x):
    return torch.nn.functional.silu(x)
def _sigmoid(x):
    return torch.sigmoid(x)
def _layernorm(x, axis=-1, eps=1e-5):
    m = x.mean(dim=axis, keepdim=True)
    v = x.var(dim=axis, keepdim=True, unbiased=False)
    return (x - m) / torch.sqrt(v + eps)
def _scatter_add(tgt, idx, src, axis=0):
    return tgt.clone().index_add_(axis, idx, src)
def _segment_sum(src, seg, n):
    out = torch.zeros((n,) + src.shape[1:], dtype=src.dtype)
    return out.index_add_(0, seg, src)
def _pad(x, pw):
    flat = tuple(v for pair in reversed(pw) for v in pair)
    return torch.nn.functional.pad(x, flat)

kernel_name = 'parallel_sum__spectral_gfnet__mixer_token_mlp__prompt'

def setup_inputs():

    _w = (lambda a: (lambda t: t.to(torch.bfloat16) if t.is_floating_point() else t)(torch.as_tensor(a)))
    def _A_setup():
        B, S, D = 1, 2048, 1024
        Df = D // 2 + 1
        return {
            "x": _w((lambda *s: torch.randn(*s, dtype=torch.bfloat16) * 0.05)(B, S, D)),
            "w_r": _w((lambda *s: torch.randn(*s, dtype=torch.bfloat16) * 1.6)(S, Df) * 0.02),
            "w_i": _w((lambda *s: torch.randn(*s, dtype=torch.bfloat16) * 1.6)(S, Df) * 0.02),
        }
    _A_in = _A_setup()

    def _B_setup():
        B, S, D, F = 1, 2048, 1024, 3584
        return {
            "x": _w((lambda *s: torch.randn(*s, dtype=torch.bfloat16) * 0.05)(B, S, D)),
            "w1": _w((lambda *s: torch.randn(*s, dtype=torch.bfloat16) * 0.4)(S, F) * (S ** -0.5)),
            "b1": _w((lambda *s: torch.randn(*s, dtype=torch.bfloat16) * 3.2)(F) * 0.02),
            "w2": _w((lambda *s: torch.randn(*s, dtype=torch.bfloat16) * 0.8)(F, S) * (F ** -0.5)),
            "b2": _w((lambda *s: torch.randn(*s, dtype=torch.bfloat16) * 3.2)(S) * 0.02),
        }
    _B_in = _B_setup()

    return {
        "A_x": _A_in["x"],
        "A_w_r": _A_in["w_r"],
        "A_w_i": _A_in["w_i"],
        "B_x": _B_in["x"],
        "B_w1": _B_in["w1"],
        "B_b1": _B_in["b1"],
        "B_w2": _B_in["w2"],
        "B_b2": _B_in["b2"],
    }

def reference(A_x, A_w_r, A_w_i, B_x, B_w1, B_b1, B_w2, B_b2):
    def _A_ref(x, w_r, w_i):
        B, S, D = x.shape
        X = (lambda x, axes=(-2,-1): torch.fft.rfft2(x.float(), dim=axes))(x, axes=(-2, -1))
        W = w_r + 1j * w_i
        X = X * W
        y = (lambda x, s, axes=(-2,-1): torch.fft.irfft2(x, s=s, dim=axes).to(torch.bfloat16))(X, (S, D), axes=(-2, -1))
        return y

    def _B_ref(x, w1, b1, w2, b2):
        B, S, D = x.shape
        h = _layernorm(x, axis=-1)
        u = torch.einsum("bsd,sf->bdf", h, w1) + b1
        u = _gelu(u)
        y = torch.einsum("bdf,fs->bds", u, w2) + b2
        return (lambda x, *d: x.permute(*d))(y, 0, 2, 1)

    x = A_x
    xn = _layernorm(x, axis=-1)
    return x + _A_ref(xn, A_w_r, A_w_i) + _B_ref(xn, B_w1, B_b1, B_w2, B_b2)
```

```cpp
#include <hip/hip_runtime.h>
#include <hip/hip_bf16.h>

typedef float f32x2 __attribute__((ext_vector_type(2)));
typedef float f32x4 __attribute__((ext_vector_type(4)));
typedef float f32x16 __attribute__((ext_vector_type(16)));
typedef unsigned u32x2 __attribute__((ext_vector_type(2)));
typedef unsigned u32x4 __attribute__((ext_vector_type(4)));
typedef unsigned u32x16 __attribute__((ext_vector_type(16)));
typedef unsigned u32x32 __attribute__((ext_vector_type(32)));
typedef short s16x4 __attribute__((ext_vector_type(4)));
typedef __bf16 bf16x2 __attribute__((ext_vector_type(2)));
typedef __bf16 bf16x8 __attribute__((ext_vector_type(8)));
#define DI __device__ __forceinline__
#define LDS __attribute__((address_space(3)))

constexpr int S = 2048, D = 1024, F = 3584;
constexpr size_t WS_H = 0;
constexpr size_t WS_T = 4u << 20;
constexpr size_t WS_TP = 8u << 20;
constexpr size_t WS_G = 12u << 20;
constexpr size_t WS_X = 20u << 20;
constexpr size_t WS_W = 28u << 20;
constexpr size_t WS_FLAG = 33u << 20;
constexpr unsigned FL_X = 0, FL_G = 131072, FL_TP = 262144, FL_H = 266240, FL_TW = 274432, FL_EPOCH = 282624;
constexpr unsigned SRD3 = 0x00020000u;

#define GEMM1_PROW \
  "s_nop 4\nbuffer_load_dwordx2 v[112:113], %[vB0], %[srdB], %[sB] offen offset:0\nbuffer_load_dwordx2 v[114:115], %[vB1], %[srdB], %[sB] offen offset:0\n" \
  "buffer_load_dwordx2 v[116:117], %[vB0], %[srdB], %[sB] offen offset:64\nbuffer_load_dwordx2 v[118:119], %[vB1], %[srdB], %[sB] offen offset:64\n" \
  "buffer_load_dwordx2 v[120:121], %[vB0], %[srdB], %[sB] offen offset:128\nbuffer_load_dwordx2 v[122:123], %[vB1], %[srdB], %[sB] offen offset:128\n" \
  "buffer_load_dwordx2 v[124:125], %[vB0], %[srdB], %[sB] offen offset:192\nbuffer_load_dwordx2 v[126:127], %[vB1], %[srdB], %[sB] offen offset:192\ns_add_u32 m0, %[ldsw], 0\n" \
  "s_nop 0\nbuffer_load_dwordx4 v208, %[srdW], %[sW] offen offset:0 lds\nbuffer_load_dwordx4 v209, %[srdW], %[sW] offen offset:1024 lds\ns_add_u32 %[sW], %[sW], %[incW]\n" \
  "s_add_u32 m0, %[ldsw], 8192\ns_nop 0\nbuffer_load_dwordx4 v208, %[srdW], %[sW] offen offset:0 lds\nbuffer_load_dwordx4 v209, %[srdW], %[sW] offen offset:1024 lds\n" \
  "s_add_u32 %[sW], %[sW], %[incW]\ns_add_u32 m0, %[ldsw], 16384\ns_nop 0\nbuffer_load_dwordx4 v208, %[srdW], %[sW] offen offset:0 lds\n" \
  "buffer_load_dwordx4 v209, %[srdW], %[sW] offen offset:1024 lds\ns_add_u32 %[sW], %[sW], %[incW]\ns_add_u32 m0, %[ldsw], 24576\ns_nop 0\n" \
  "buffer_load_dwordx4 v208, %[srdW], %[sW] offen offset:0 lds\nbuffer_load_dwordx4 v209, %[srdW], %[sW] offen offset:1024 lds\ns_add_u32 %[sW], %[sW], %[incW]\n" \
  "s_add_u32 m0, %[ldsw], 32768\ns_nop 0\nbuffer_load_dwordx4 v208, %[srdW], %[sW] offen offset:0 lds\nbuffer_load_dwordx4 v209, %[srdW], %[sW] offen offset:1024 lds\n" \
  "s_add_u32 %[sW], %[sW], %[incW]\ns_add_u32 m0, %[ldsw], 40960\ns_nop 0\nbuffer_load_dwordx4 v208, %[srdW], %[sW] offen offset:0 lds\n" \
  "buffer_load_dwordx4 v209, %[srdW], %[sW] offen offset:1024 lds\ns_add_u32 %[sW], %[sW], %[incW]\n"
#define GEMM1_PRO \
  "s_nop 4\nbuffer_load_dword v234, %[vN], %[srdH], 0 offen\nbuffer_load_dword v234, %[vN], %[srdH], 0 offen\n" \
  "buffer_load_dwordx4 v[144:147], %[vH], %[srdH], %[sH] offen offset:0 sc1\nbuffer_load_dwordx4 v[148:151], %[vH], %[srdH], %[sH] offen offset:1024 sc1\n" \
  "s_add_u32 %[sH], %[sH], 8192\nbuffer_load_dword v234, %[vN], %[srdH], 0 offen\nbuffer_load_dword v234, %[vN], %[srdH], 0 offen\n" \
  "buffer_load_dwordx4 v[152:155], %[vH], %[srdH], %[sH] offen offset:0 sc1\nbuffer_load_dwordx4 v[156:159], %[vH], %[srdH], %[sH] offen offset:1024 sc1\n" \
  "s_add_u32 %[sH], %[sH], 8192\nbuffer_load_dword v234, %[vN], %[srdH], 0 offen\nbuffer_load_dword v234, %[vN], %[srdH], 0 offen\n" \
  "buffer_load_dwordx4 v[160:163], %[vH], %[srdH], %[sH] offen offset:0 sc1\nbuffer_load_dwordx4 v[164:167], %[vH], %[srdH], %[sH] offen offset:1024 sc1\n" \
  "s_add_u32 %[sH], %[sH], 8192\nbuffer_load_dword v234, %[vN], %[srdH], 0 offen\nbuffer_load_dword v234, %[vN], %[srdH], 0 offen\n" \
  "buffer_load_dwordx4 v[168:171], %[vH], %[srdH], %[sH] offen offset:0 sc1\nbuffer_load_dwordx4 v[172:175], %[vH], %[srdH], %[sH] offen offset:1024 sc1\n" \
  "s_add_u32 %[sH], %[sH], 8192\nbuffer_load_dword v234, %[vN], %[srdH], 0 offen\nbuffer_load_dword v234, %[vN], %[srdH], 0 offen\n" \
  "buffer_load_dwordx4 v[176:179], %[vH], %[srdH], %[sH] offen offset:0 sc1\nbuffer_load_dwordx4 v[180:183], %[vH], %[srdH], %[sH] offen offset:1024 sc1\n" \
  "s_add_u32 %[sH], %[sH], 8192\nbuffer_load_dword v234, %[vN], %[srdH], 0 offen\nbuffer_load_dword v234, %[vN], %[srdH], 0 offen\n" \
  "buffer_load_dwordx4 v[184:187], %[vH], %[srdH], %[sH] offen offset:0 sc1\nbuffer_load_dwordx4 v[188:191], %[vH], %[srdH], %[sH] offen offset:1024 sc1\n" \
  "s_add_u32 %[sH], %[sH], 8192\n"
#define GEMM1_ASM \
  "s_waitcnt vmcnt(24) lgkmcnt(0)\ns_barrier\nds_read_b64_tr_b16 v[80:81], v210 offset:0\nds_read_b64_tr_b16 v[82:83], v211 offset:0\nds_read_b64_tr_b16 v[84:85], v212 offset:0\n" \
  "ds_read_b64_tr_b16 v[86:87], v213 offset:0\nds_read_b64_tr_b16 v[88:89], v214 offset:0\nds_read_b64_tr_b16 v[90:91], v215 offset:0\nds_read_b64_tr_b16 v[92:93], v216 offset:0\n" \
  "ds_read_b64_tr_b16 v[94:95], v217 offset:0\nds_read_b64_tr_b16 v[96:97], v218 offset:0\nds_read_b64_tr_b16 v[98:99], v219 offset:0\nds_read_b64_tr_b16 v[100:101], v220 offset:0\n" \
  "ds_read_b64_tr_b16 v[102:103], v221 offset:0\nds_read_b64_tr_b16 v[104:105], v222 offset:0\nds_read_b64_tr_b16 v[106:107], v223 offset:0\n" \
  "ds_read_b64_tr_b16 v[108:109], v224 offset:0\nds_read_b64_tr_b16 v[110:111], v225 offset:0\nv_lshlrev_b32 v16, 16, v112\nv_lshlrev_b32 v48, 16, v112\n" \
  "v_and_b32 v17, 0xffff0000, v112\nv_and_b32 v49, 0xffff0000, v112\nv_lshlrev_b32 v18, 16, v113\nv_lshlrev_b32 v50, 16, v113\nv_and_b32 v19, 0xffff0000, v113\n" \
  "v_and_b32 v51, 0xffff0000, v113\nv_lshlrev_b32 v20, 16, v114\nv_lshlrev_b32 v52, 16, v114\nv_and_b32 v21, 0xffff0000, v114\nv_and_b32 v53, 0xffff0000, v114\n" \
  "v_lshlrev_b32 v22, 16, v115\nv_lshlrev_b32 v54, 16, v115\nv_and_b32 v23, 0xffff0000, v115\nv_and_b32 v55, 0xffff0000, v115\nv_lshlrev_b32 v24, 16, v116\n" \
  "v_lshlrev_b32 v56, 16, v116\nv_and_b32 v25, 0xffff0000, v116\nv_and_b32 v57, 0xffff0000, v116\nv_lshlrev_b32 v26, 16, v117\nv_lshlrev_b32 v58, 16, v117\n" \
  "v_and_b32 v27, 0xffff0000, v117\nv_and_b32 v59, 0xffff0000, v117\nv_lshlrev_b32 v28, 16, v118\nv_lshlrev_b32 v60, 16, v118\nv_and_b32 v29, 0xffff0000, v118\n" \
  "v_and_b32 v61, 0xffff0000, v118\nv_lshlrev_b32 v30, 16, v119\nv_lshlrev_b32 v62, 16, v119\nv_and_b32 v31, 0xffff0000, v119\nv_and_b32 v63, 0xffff0000, v119\n" \
  "v_lshlrev_b32 v32, 16, v120\nv_lshlrev_b32 v64, 16, v120\nv_and_b32 v33, 0xffff0000, v120\nv_and_b32 v65, 0xffff0000, v120\nv_lshlrev_b32 v34, 16, v121\n" \
  "v_lshlrev_b32 v66, 16, v121\nv_and_b32 v35, 0xffff0000, v121\nv_and_b32 v67, 0xffff0000, v121\nv_lshlrev_b32 v36, 16, v122\nv_lshlrev_b32 v68, 16, v122\n" \
  "v_and_b32 v37, 0xffff0000, v122\nv_and_b32 v69, 0xffff0000, v122\nv_lshlrev_b32 v38, 16, v123\nv_lshlrev_b32 v70, 16, v123\nv_and_b32 v39, 0xffff0000, v123\n" \
  "v_and_b32 v71, 0xffff0000, v123\nv_lshlrev_b32 v40, 16, v124\nv_lshlrev_b32 v72, 16, v124\nv_and_b32 v41, 0xffff0000, v124\nv_and_b32 v73, 0xffff0000, v124\n" \
  "v_lshlrev_b32 v42, 16, v125\nv_lshlrev_b32 v74, 16, v125\nv_and_b32 v43, 0xffff0000, v125\nv_and_b32 v75, 0xffff0000, v125\nv_lshlrev_b32 v44, 16, v126\n" \
  "v_lshlrev_b32 v76, 16, v126\nv_and_b32 v45, 0xffff0000, v126\nv_and_b32 v77, 0xffff0000, v126\nv_lshlrev_b32 v46, 16, v127\nv_lshlrev_b32 v78, 16, v127\n" \
  "v_and_b32 v47, 0xffff0000, v127\nv_and_b32 v79, 0xffff0000, v127\ns_mov_b32 %[cnt], 7\nLk%=:\ns_waitcnt vmcnt(20)\ns_waitcnt lgkmcnt(8)\n" \
  "v_mfma_f32_16x16x32_bf16 v[16:19], v[80:83], v[144:147], v[16:19]\nds_read_b64_tr_b16 v[112:113], v210 offset:8192\n" \
  "v_mfma_f32_16x16x32_bf16 v[48:51], v[80:83], v[148:151], v[48:51]\nds_read_b64_tr_b16 v[114:115], v211 offset:8192\ns_add_u32 m0, %[ldsw], 49152\n" \
  "v_mfma_f32_16x16x32_bf16 v[20:23], v[84:87], v[144:147], v[20:23]\nds_read_b64_tr_b16 v[116:117], v212 offset:8192\n" \
  "v_mfma_f32_16x16x32_bf16 v[52:55], v[84:87], v[148:151], v[52:55]\nds_read_b64_tr_b16 v[118:119], v213 offset:8192\nbuffer_load_dwordx4 v208, %[srdW], %[sW] offen offset:0 lds\n" \
  "v_mfma_f32_16x16x32_bf16 v[24:27], v[88:91], v[144:147], v[24:27]\nds_read_b64_tr_b16 v[120:121], v214 offset:8192\n" \
  "v_mfma_f32_16x16x32_bf16 v[56:59], v[88:91], v[148:151], v[56:59]\nds_read_b64_tr_b16 v[122:123], v215 offset:8192\n" \
  "v_mfma_f32_16x16x32_bf16 v[28:31], v[92:95], v[144:147], v[28:31]\nds_read_b64_tr_b16 v[124:125], v216 offset:8192\ns_cmp_eq_u32 %[cnt], %[tcnt]\ns_cbranch_scc0 Lt%=\n" \
  "buffer_load_dword v234, %[vT], %[srdW], 0 offen\nbuffer_load_dword v234, %[vT2], %[srdW], 0 offen\nLt%=:\nv_mfma_f32_16x16x32_bf16 v[60:63], v[92:95], v[148:151], v[60:63]\n" \
  "ds_read_b64_tr_b16 v[126:127], v217 offset:8192\nbuffer_load_dwordx4 v209, %[srdW], %[sW] offen offset:1024 lds\ns_waitcnt lgkmcnt(8)\n" \
  "v_mfma_f32_16x16x32_bf16 v[32:35], v[96:99], v[144:147], v[32:35]\nds_read_b64_tr_b16 v[128:129], v218 offset:8192\n" \
  "v_mfma_f32_16x16x32_bf16 v[64:67], v[96:99], v[148:151], v[64:67]\nds_read_b64_tr_b16 v[130:131], v219 offset:8192\ns_add_u32 %[sW], %[sW], %[incW]\n" \
  "v_mfma_f32_16x16x32_bf16 v[36:39], v[100:103], v[144:147], v[36:39]\nds_read_b64_tr_b16 v[132:133], v220 offset:8192\n" \
  "v_mfma_f32_16x16x32_bf16 v[68:71], v[100:103], v[148:151], v[68:71]\nds_read_b64_tr_b16 v[134:135], v221 offset:8192\n" \
  "buffer_load_dwordx4 v[192:195], %[vH], %[srdH], %[sH] offen offset:0 sc1\nv_mfma_f32_16x16x32_bf16 v[40:43], v[104:107], v[144:147], v[40:43]\n" \
  "ds_read_b64_tr_b16 v[136:137], v222 offset:8192\nv_mfma_f32_16x16x32_bf16 v[72:75], v[104:107], v[148:151], v[72:75]\nds_read_b64_tr_b16 v[138:139], v223 offset:8192\n" \
  "v_mfma_f32_16x16x32_bf16 v[44:47], v[108:111], v[144:147], v[44:47]\nds_read_b64_tr_b16 v[140:141], v224 offset:8192\n" \
  "v_mfma_f32_16x16x32_bf16 v[76:79], v[108:111], v[148:151], v[76:79]\nds_read_b64_tr_b16 v[142:143], v225 offset:8192\n" \
  "buffer_load_dwordx4 v[196:199], %[vH], %[srdH], %[sH] offen offset:1024 sc1\ns_add_u32 %[sH], %[sH], 8192\ns_waitcnt vmcnt(14)\ns_barrier\ns_waitcnt lgkmcnt(8)\n" \
  "v_mfma_f32_16x16x32_bf16 v[16:19], v[112:115], v[152:155], v[16:19]\nds_read_b64_tr_b16 v[80:81], v210 offset:16384\n" \
  "v_mfma_f32_16x16x32_bf16 v[48:51], v[112:115], v[156:159], v[48:51]\nds_read_b64_tr_b16 v[82:83], v211 offset:16384\ns_add_u32 m0, %[ldsw], 57344\n" \
  "v_mfma_f32_16x16x32_bf16 v[20:23], v[116:119], v[152:155], v[20:23]\nds_read_b64_tr_b16 v[84:85], v212 offset:16384\n" \
  "v_mfma_f32_16x16x32_bf16 v[52:55], v[116:119], v[156:159], v[52:55]\nds_read_b64_tr_b16 v[86:87], v213 offset:16384\nbuffer_load_dwordx4 v208, %[srdW], %[sW] offen offset:0 lds\n" \
  "v_mfma_f32_16x16x32_bf16 v[24:27], v[120:123], v[152:155], v[24:27]\nds_read_b64_tr_b16 v[88:89], v214 offset:16384\n" \
  "v_mfma_f32_16x16x32_bf16 v[56:59], v[120:123], v[156:159], v[56:59]\nds_read_b64_tr_b16 v[90:91], v215 offset:16384\n" \
  "v_mfma_f32_16x16x32_bf16 v[28:31], v[124:127], v[152:155], v[28:31]\nds_read_b64_tr_b16 v[92:93], v216 offset:16384\n" \
  "v_mfma_f32_16x16x32_bf16 v[60:63], v[124:127], v[156:159], v[60:63]\nds_read_b64_tr_b16 v[94:95], v217 offset:16384\n" \
  "buffer_load_dwordx4 v209, %[srdW], %[sW] offen offset:1024 lds\ns_waitcnt lgkmcnt(8)\nv_mfma_f32_16x16x32_bf16 v[32:35], v[128:131], v[152:155], v[32:35]\n" \
  "ds_read_b64_tr_b16 v[96:97], v218 offset:16384\nv_mfma_f32_16x16x32_bf16 v[64:67], v[128:131], v[156:159], v[64:67]\nds_read_b64_tr_b16 v[98:99], v219 offset:16384\n" \
  "s_add_u32 %[sW], %[sW], %[jmp]\nv_mfma_f32_16x16x32_bf16 v[36:39], v[132:135], v[152:155], v[36:39]\nds_read_b64_tr_b16 v[100:101], v220 offset:16384\n" \
  "v_mfma_f32_16x16x32_bf16 v[68:71], v[132:135], v[156:159], v[68:71]\nds_read_b64_tr_b16 v[102:103], v221 offset:16384\n" \
  "buffer_load_dwordx4 v[200:203], %[vH], %[srdH], %[sH] offen offset:0 sc1\nv_mfma_f32_16x16x32_bf16 v[40:43], v[136:139], v[152:155], v[40:43]\n" \
  "ds_read_b64_tr_b16 v[104:105], v222 offset:16384\nv_mfma_f32_16x16x32_bf16 v[72:75], v[136:139], v[156:159], v[72:75]\nds_read_b64_tr_b16 v[106:107], v223 offset:16384\n" \
  "v_mfma_f32_16x16x32_bf16 v[44:47], v[140:143], v[152:155], v[44:47]\nds_read_b64_tr_b16 v[108:109], v224 offset:16384\n" \
  "v_mfma_f32_16x16x32_bf16 v[76:79], v[140:143], v[156:159], v[76:79]\nds_read_b64_tr_b16 v[110:111], v225 offset:16384\n" \
  "buffer_load_dwordx4 v[204:207], %[vH], %[srdH], %[sH] offen offset:1024 sc1\ns_add_u32 %[sH], %[sH], 8192\ns_waitcnt vmcnt(20)\ns_waitcnt lgkmcnt(8)\n" \
  "v_mfma_f32_16x16x32_bf16 v[16:19], v[80:83], v[160:163], v[16:19]\nds_read_b64_tr_b16 v[112:113], v210 offset:24576\n" \
  "v_mfma_f32_16x16x32_bf16 v[48:51], v[80:83], v[164:167], v[48:51]\nds_read_b64_tr_b16 v[114:115], v211 offset:24576\ns_add_u32 m0, %[ldsw], 0\n" \
  "v_mfma_f32_16x16x32_bf16 v[20:23], v[84:87], v[160:163], v[20:23]\nds_read_b64_tr_b16 v[116:117], v212 offset:24576\n" \
  "v_mfma_f32_16x16x32_bf16 v[52:55], v[84:87], v[164:167], v[52:55]\nds_read_b64_tr_b16 v[118:119], v213 offset:24576\nbuffer_load_dwordx4 v208, %[srdW], %[sW] offen offset:0 lds\n" \
  "v_mfma_f32_16x16x32_bf16 v[24:27], v[88:91], v[160:163], v[24:27]\nds_read_b64_tr_b16 v[120:121], v214 offset:24576\n" \
  "v_mfma_f32_16x16x32_bf16 v[56:59], v[88:91], v[164:167], v[56:59]\nds_read_b64_tr_b16 v[122:123], v215 offset:24576\n" \
  "v_mfma_f32_16x16x32_bf16 v[28:31], v[92:95], v[160:163], v[28:31]\nds_read_b64_tr_b16 v[124:125], v216 offset:24576\n" \
  "v_mfma_f32_16x16x32_bf16 v[60:63], v[92:95], v[164:167], v[60:63]\nds_read_b64_tr_b16 v[126:127], v217 offset:24576\n" \
  "buffer_load_dwordx4 v209, %[srdW], %[sW] offen offset:1024 lds\ns_waitcnt lgkmcnt(8)\nv_mfma_f32_16x16x32_bf16 v[32:35], v[96:99], v[160:163], v[32:35]\n" \
  "ds_read_b64_tr_b16 v[128:129], v218 offset:24576\nv_mfma_f32_16x16x32_bf16 v[64:67], v[96:99], v[164:167], v[64:67]\nds_read_b64_tr_b16 v[130:131], v219 offset:24576\n" \
  "s_add_u32 %[sW], %[sW], %[incW]\nv_mfma_f32_16x16x32_bf16 v[36:39], v[100:103], v[160:163], v[36:39]\nds_read_b64_tr_b16 v[132:133], v220 offset:24576\n" \
  "v_mfma_f32_16x16x32_bf16 v[68:71], v[100:103], v[164:167], v[68:71]\nds_read_b64_tr_b16 v[134:135], v221 offset:24576\n" \
  "buffer_load_dwordx4 v[144:147], %[vH], %[srdH], %[sH] offen offset:0 sc1\nv_mfma_f32_16x16x32_bf16 v[40:43], v[104:107], v[160:163], v[40:43]\n" \
  "ds_read_b64_tr_b16 v[136:137], v222 offset:24576\nv_mfma_f32_16x16x32_bf16 v[72:75], v[104:107], v[164:167], v[72:75]\nds_read_b64_tr_b16 v[138:139], v223 offset:24576\n" \
  "v_mfma_f32_16x16x32_bf16 v[44:47], v[108:111], v[160:163], v[44:47]\nds_read_b64_tr_b16 v[140:141], v224 offset:24576\n" \
  "v_mfma_f32_16x16x32_bf16 v[76:79], v[108:111], v[164:167], v[76:79]\nds_read_b64_tr_b16 v[142:143], v225 offset:24576\n" \
  "buffer_load_dwordx4 v[148:151], %[vH], %[srdH], %[sH] offen offset:1024 sc1\ns_add_u32 %[sH], %[sH], 8192\ns_waitcnt vmcnt(14)\ns_barrier\ns_waitcnt lgkmcnt(8)\n" \
  "v_mfma_f32_16x16x32_bf16 v[16:19], v[112:115], v[168:171], v[16:19]\nds_read_b64_tr_b16 v[80:81], v210 offset:32768\n" \
  "v_mfma_f32_16x16x32_bf16 v[48:51], v[112:115], v[172:175], v[48:51]\nds_read_b64_tr_b16 v[82:83], v211 offset:32768\ns_add_u32 m0, %[ldsw], 8192\n" \
  "v_mfma_f32_16x16x32_bf16 v[20:23], v[116:119], v[168:171], v[20:23]\nds_read_b64_tr_b16 v[84:85], v212 offset:32768\n" \
  "v_mfma_f32_16x16x32_bf16 v[52:55], v[116:119], v[172:175], v[52:55]\nds_read_b64_tr_b16 v[86:87], v213 offset:32768\nbuffer_load_dwordx4 v208, %[srdW], %[sW] offen offset:0 lds\n" \
  "v_mfma_f32_16x16x32_bf16 v[24:27], v[120:123], v[168:171], v[24:27]\nds_read_b64_tr_b16 v[88:89], v214 offset:32768\n" \
  "v_mfma_f32_16x16x32_bf16 v[56:59], v[120:123], v[172:175], v[56:59]\nds_read_b64_tr_b16 v[90:91], v215 offset:32768\n" \
  "v_mfma_f32_16x16x32_bf16 v[28:31], v[124:127], v[168:171], v[28:31]\nds_read_b64_tr_b16 v[92:93], v216 offset:32768\n" \
  "v_mfma_f32_16x16x32_bf16 v[60:63], v[124:127], v[172:175], v[60:63]\nds_read_b64_tr_b16 v[94:95], v217 offset:32768\n" \
  "buffer_load_dwordx4 v209, %[srdW], %[sW] offen offset:1024 lds\ns_waitcnt lgkmcnt(8)\nv_mfma_f32_16x16x32_bf16 v[32:35], v[128:131], v[168:171], v[32:35]\n" \
  "ds_read_b64_tr_b16 v[96:97], v218 offset:32768\nv_mfma_f32_16x16x32_bf16 v[64:67], v[128:131], v[172:175], v[64:67]\nds_read_b64_tr_b16 v[98:99], v219 offset:32768\n" \
  "s_add_u32 %[sW], %[sW], %[incW]\nv_mfma_f32_16x16x32_bf16 v[36:39], v[132:135], v[168:171], v[36:39]\nds_read_b64_tr_b16 v[100:101], v220 offset:32768\n" \
  "v_mfma_f32_16x16x32_bf16 v[68:71], v[132:135], v[172:175], v[68:71]\nds_read_b64_tr_b16 v[102:103], v221 offset:32768\n" \
  "buffer_load_dwordx4 v[152:155], %[vH], %[srdH], %[sH] offen offset:0 sc1\nv_mfma_f32_16x16x32_bf16 v[40:43], v[136:139], v[168:171], v[40:43]\n" \
  "ds_read_b64_tr_b16 v[104:105], v222 offset:32768\nv_mfma_f32_16x16x32_bf16 v[72:75], v[136:139], v[172:175], v[72:75]\nds_read_b64_tr_b16 v[106:107], v223 offset:32768\n" \
  "v_mfma_f32_16x16x32_bf16 v[44:47], v[140:143], v[168:171], v[44:47]\nds_read_b64_tr_b16 v[108:109], v224 offset:32768\n" \
  "v_mfma_f32_16x16x32_bf16 v[76:79], v[140:143], v[172:175], v[76:79]\nds_read_b64_tr_b16 v[110:111], v225 offset:32768\n" \
  "buffer_load_dwordx4 v[156:159], %[vH], %[srdH], %[sH] offen offset:1024 sc1\ns_add_u32 %[sH], %[sH], 8192\ns_waitcnt vmcnt(20)\ns_waitcnt lgkmcnt(8)\n" \
  "v_mfma_f32_16x16x32_bf16 v[16:19], v[80:83], v[176:179], v[16:19]\nds_read_b64_tr_b16 v[112:113], v210 offset:40960\n" \
  "v_mfma_f32_16x16x32_bf16 v[48:51], v[80:83], v[180:183], v[48:51]\nds_read_b64_tr_b16 v[114:115], v211 offset:40960\ns_add_u32 m0, %[ldsw], 16384\n" \
  "v_mfma_f32_16x16x32_bf16 v[20:23], v[84:87], v[176:179], v[20:23]\nds_read_b64_tr_b16 v[116:117], v212 offset:40960\n" \
  "v_mfma_f32_16x16x32_bf16 v[52:55], v[84:87], v[180:183], v[52:55]\nds_read_b64_tr_b16 v[118:119], v213 offset:40960\nbuffer_load_dwordx4 v208, %[srdW], %[sW] offen offset:0 lds\n" \
  "v_mfma_f32_16x16x32_bf16 v[24:27], v[88:91], v[176:179], v[24:27]\nds_read_b64_tr_b16 v[120:121], v214 offset:40960\n" \
  "v_mfma_f32_16x16x32_bf16 v[56:59], v[88:91], v[180:183], v[56:59]\nds_read_b64_tr_b16 v[122:123], v215 offset:40960\n" \
  "v_mfma_f32_16x16x32_bf16 v[28:31], v[92:95], v[176:179], v[28:31]\nds_read_b64_tr_b16 v[124:125], v216 offset:40960\n" \
  "v_mfma_f32_16x16x32_bf16 v[60:63], v[92:95], v[180:183], v[60:63]\nds_read_b64_tr_b16 v[126:127], v217 offset:40960\n" \
  "buffer_load_dwordx4 v209, %[srdW], %[sW] offen offset:1024 lds\ns_waitcnt lgkmcnt(8)\nv_mfma_f32_16x16x32_bf16 v[32:35], v[96:99], v[176:179], v[32:35]\n" \
  "ds_read_b64_tr_b16 v[128:129], v218 offset:40960\nv_mfma_f32_16x16x32_bf16 v[64:67], v[96:99], v[180:183], v[64:67]\nds_read_b64_tr_b16 v[130:131], v219 offset:40960\n" \
  "s_add_u32 %[sW], %[sW], %[incW]\nv_mfma_f32_16x16x32_bf16 v[36:39], v[100:103], v[176:179], v[36:39]\nds_read_b64_tr_b16 v[132:133], v220 offset:40960\n" \
  "v_mfma_f32_16x16x32_bf16 v[68:71], v[100:103], v[180:183], v[68:71]\nds_read_b64_tr_b16 v[134:135], v221 offset:40960\n" \
  "buffer_load_dwordx4 v[160:163], %[vH], %[srdH], %[sH] offen offset:0 sc1\nv_mfma_f32_16x16x32_bf16 v[40:43], v[104:107], v[176:179], v[40:43]\n" \
  "ds_read_b64_tr_b16 v[136:137], v222 offset:40960\nv_mfma_f32_16x16x32_bf16 v[72:75], v[104:107], v[180:183], v[72:75]\nds_read_b64_tr_b16 v[138:139], v223 offset:40960\n" \
  "v_mfma_f32_16x16x32_bf16 v[44:47], v[108:111], v[176:179], v[44:47]\nds_read_b64_tr_b16 v[140:141], v224 offset:40960\n" \
  "v_mfma_f32_16x16x32_bf16 v[76:79], v[108:111], v[180:183], v[76:79]\nds_read_b64_tr_b16 v[142:143], v225 offset:40960\n" \
  "buffer_load_dwordx4 v[164:167], %[vH], %[srdH], %[sH] offen offset:1024 sc1\ns_add_u32 %[sH], %[sH], 8192\ns_waitcnt vmcnt(14)\ns_barrier\ns_waitcnt lgkmcnt(8)\n" \
  "v_mfma_f32_16x16x32_bf16 v[16:19], v[112:115], v[184:187], v[16:19]\nds_read_b64_tr_b16 v[80:81], v210 offset:49152\n" \
  "v_mfma_f32_16x16x32_bf16 v[48:51], v[112:115], v[188:191], v[48:51]\nds_read_b64_tr_b16 v[82:83], v211 offset:49152\ns_add_u32 m0, %[ldsw], 24576\n" \
  "v_mfma_f32_16x16x32_bf16 v[20:23], v[116:119], v[184:187], v[20:23]\nds_read_b64_tr_b16 v[84:85], v212 offset:49152\n" \
  "v_mfma_f32_16x16x32_bf16 v[52:55], v[116:119], v[188:191], v[52:55]\nds_read_b64_tr_b16 v[86:87], v213 offset:49152\nbuffer_load_dwordx4 v208, %[srdW], %[sW] offen offset:0 lds\n" \
  "v_mfma_f32_16x16x32_bf16 v[24:27], v[120:123], v[184:187], v[24:27]\nds_read_b64_tr_b16 v[88:89], v214 offset:49152\n" \
  "v_mfma_f32_16x16x32_bf16 v[56:59], v[120:123], v[188:191], v[56:59]\nds_read_b64_tr_b16 v[90:91], v215 offset:49152\n" \
  "v_mfma_f32_16x16x32_bf16 v[28:31], v[124:127], v[184:187], v[28:31]\nds_read_b64_tr_b16 v[92:93], v216 offset:49152\n" \
  "v_mfma_f32_16x16x32_bf16 v[60:63], v[124:127], v[188:191], v[60:63]\nds_read_b64_tr_b16 v[94:95], v217 offset:49152\n" \
  "buffer_load_dwordx4 v209, %[srdW], %[sW] offen offset:1024 lds\ns_waitcnt lgkmcnt(8)\nv_mfma_f32_16x16x32_bf16 v[32:35], v[128:131], v[184:187], v[32:35]\n" \
  "ds_read_b64_tr_b16 v[96:97], v218 offset:49152\nv_mfma_f32_16x16x32_bf16 v[64:67], v[128:131], v[188:191], v[64:67]\nds_read_b64_tr_b16 v[98:99], v219 offset:49152\n" \
  "s_add_u32 %[sW], %[sW], %[incW]\nv_mfma_f32_16x16x32_bf16 v[36:39], v[132:135], v[184:187], v[36:39]\nds_read_b64_tr_b16 v[100:101], v220 offset:49152\n" \
  "v_mfma_f32_16x16x32_bf16 v[68:71], v[132:135], v[188:191], v[68:71]\nds_read_b64_tr_b16 v[102:103], v221 offset:49152\n" \
  "buffer_load_dwordx4 v[168:171], %[vH], %[srdH], %[sH] offen offset:0 sc1\nv_mfma_f32_16x16x32_bf16 v[40:43], v[136:139], v[184:187], v[40:43]\n" \
  "ds_read_b64_tr_b16 v[104:105], v222 offset:49152\nv_mfma_f32_16x16x32_bf16 v[72:75], v[136:139], v[188:191], v[72:75]\nds_read_b64_tr_b16 v[106:107], v223 offset:49152\n" \
  "v_mfma_f32_16x16x32_bf16 v[44:47], v[140:143], v[184:187], v[44:47]\nds_read_b64_tr_b16 v[108:109], v224 offset:49152\n" \
  "v_mfma_f32_16x16x32_bf16 v[76:79], v[140:143], v[188:191], v[76:79]\nds_read_b64_tr_b16 v[110:111], v225 offset:49152\n" \
  "buffer_load_dwordx4 v[172:175], %[vH], %[srdH], %[sH] offen offset:1024 sc1\ns_add_u32 %[sH], %[sH], 8192\ns_waitcnt vmcnt(20)\ns_waitcnt lgkmcnt(8)\n" \
  "v_mfma_f32_16x16x32_bf16 v[16:19], v[80:83], v[192:195], v[16:19]\nds_read_b64_tr_b16 v[112:113], v210 offset:57344\n" \
  "v_mfma_f32_16x16x32_bf16 v[48:51], v[80:83], v[196:199], v[48:51]\nds_read_b64_tr_b16 v[114:115], v211 offset:57344\ns_add_u32 m0, %[ldsw], 32768\n" \
  "v_mfma_f32_16x16x32_bf16 v[20:23], v[84:87], v[192:195], v[20:23]\nds_read_b64_tr_b16 v[116:117], v212 offset:57344\n" \
  "v_mfma_f32_16x16x32_bf16 v[52:55], v[84:87], v[196:199], v[52:55]\nds_read_b64_tr_b16 v[118:119], v213 offset:57344\nbuffer_load_dwordx4 v208, %[srdW], %[sW] offen offset:0 lds\n" \
  "v_mfma_f32_16x16x32_bf16 v[24:27], v[88:91], v[192:195], v[24:27]\nds_read_b64_tr_b16 v[120:121], v214 offset:57344\n" \
  "v_mfma_f32_16x16x32_bf16 v[56:59], v[88:91], v[196:199], v[56:59]\nds_read_b64_tr_b16 v[122:123], v215 offset:57344\n" \
  "v_mfma_f32_16x16x32_bf16 v[28:31], v[92:95], v[192:195], v[28:31]\nds_read_b64_tr_b16 v[124:125], v216 offset:57344\n" \
  "v_mfma_f32_16x16x32_bf16 v[60:63], v[92:95], v[196:199], v[60:63]\nds_read_b64_tr_b16 v[126:127], v217 offset:57344\n" \
  "buffer_load_dwordx4 v209, %[srdW], %[sW] offen offset:1024 lds\ns_waitcnt lgkmcnt(8)\nv_mfma_f32_16x16x32_bf16 v[32:35], v[96:99], v[192:195], v[32:35]\n" \
  "ds_read_b64_tr_b16 v[128:129], v218 offset:57344\nv_mfma_f32_16x16x32_bf16 v[64:67], v[96:99], v[196:199], v[64:67]\nds_read_b64_tr_b16 v[130:131], v219 offset:57344\n" \
  "s_add_u32 %[sW], %[sW], %[incW]\nv_mfma_f32_16x16x32_bf16 v[36:39], v[100:103], v[192:195], v[36:39]\nds_read_b64_tr_b16 v[132:133], v220 offset:57344\n" \
  "v_mfma_f32_16x16x32_bf16 v[68:71], v[100:103], v[196:199], v[68:71]\nds_read_b64_tr_b16 v[134:135], v221 offset:57344\n" \
  "buffer_load_dwordx4 v[176:179], %[vH], %[srdH], %[sH] offen offset:0 sc1\nv_mfma_f32_16x16x32_bf16 v[40:43], v[104:107], v[192:195], v[40:43]\n" \
  "ds_read_b64_tr_b16 v[136:137], v222 offset:57344\nv_mfma_f32_16x16x32_bf16 v[72:75], v[104:107], v[196:199], v[72:75]\nds_read_b64_tr_b16 v[138:139], v223 offset:57344\n" \
  "v_mfma_f32_16x16x32_bf16 v[44:47], v[108:111], v[192:195], v[44:47]\nds_read_b64_tr_b16 v[140:141], v224 offset:57344\n" \
  "v_mfma_f32_16x16x32_bf16 v[76:79], v[108:111], v[196:199], v[76:79]\nds_read_b64_tr_b16 v[142:143], v225 offset:57344\n" \
  "buffer_load_dwordx4 v[180:183], %[vH], %[srdH], %[sH] offen offset:1024 sc1\ns_add_u32 %[sH], %[sH], 8192\ns_waitcnt vmcnt(14)\ns_barrier\ns_waitcnt lgkmcnt(8)\n" \
  "v_mfma_f32_16x16x32_bf16 v[16:19], v[112:115], v[200:203], v[16:19]\nds_read_b64_tr_b16 v[80:81], v210 offset:0\n" \
  "v_mfma_f32_16x16x32_bf16 v[48:51], v[112:115], v[204:207], v[48:51]\nds_read_b64_tr_b16 v[82:83], v211 offset:0\ns_add_u32 m0, %[ldsw], 40960\n" \
  "v_mfma_f32_16x16x32_bf16 v[20:23], v[116:119], v[200:203], v[20:23]\nds_read_b64_tr_b16 v[84:85], v212 offset:0\n" \
  "v_mfma_f32_16x16x32_bf16 v[52:55], v[116:119], v[204:207], v[52:55]\nds_read_b64_tr_b16 v[86:87], v213 offset:0\nbuffer_load_dwordx4 v208, %[srdW], %[sW] offen offset:0 lds\n" \
  "v_mfma_f32_16x16x32_bf16 v[24:27], v[120:123], v[200:203], v[24:27]\nds_read_b64_tr_b16 v[88:89], v214 offset:0\n" \
  "v_mfma_f32_16x16x32_bf16 v[56:59], v[120:123], v[204:207], v[56:59]\nds_read_b64_tr_b16 v[90:91], v215 offset:0\n" \
  "v_mfma_f32_16x16x32_bf16 v[28:31], v[124:127], v[200:203], v[28:31]\nds_read_b64_tr_b16 v[92:93], v216 offset:0\n" \
  "v_mfma_f32_16x16x32_bf16 v[60:63], v[124:127], v[204:207], v[60:63]\nds_read_b64_tr_b16 v[94:95], v217 offset:0\nbuffer_load_dwordx4 v209, %[srdW], %[sW] offen offset:1024 lds\n" \
  "s_waitcnt lgkmcnt(8)\nv_mfma_f32_16x16x32_bf16 v[32:35], v[128:131], v[200:203], v[32:35]\nds_read_b64_tr_b16 v[96:97], v218 offset:0\n" \
  "v_mfma_f32_16x16x32_bf16 v[64:67], v[128:131], v[204:207], v[64:67]\nds_read_b64_tr_b16 v[98:99], v219 offset:0\ns_add_u32 %[sW], %[sW], %[incW]\n" \
  "v_mfma_f32_16x16x32_bf16 v[36:39], v[132:135], v[200:203], v[36:39]\nds_read_b64_tr_b16 v[100:101], v220 offset:0\n" \
  "v_mfma_f32_16x16x32_bf16 v[68:71], v[132:135], v[204:207], v[68:71]\nds_read_b64_tr_b16 v[102:103], v221 offset:0\n" \
  "buffer_load_dwordx4 v[184:187], %[vH], %[srdH], %[sH] offen offset:0 sc1\nv_mfma_f32_16x16x32_bf16 v[40:43], v[136:139], v[200:203], v[40:43]\n" \
  "ds_read_b64_tr_b16 v[104:105], v222 offset:0\nv_mfma_f32_16x16x32_bf16 v[72:75], v[136:139], v[204:207], v[72:75]\nds_read_b64_tr_b16 v[106:107], v223 offset:0\n" \
  "v_mfma_f32_16x16x32_bf16 v[44:47], v[140:143], v[200:203], v[44:47]\nds_read_b64_tr_b16 v[108:109], v224 offset:0\n" \
  "v_mfma_f32_16x16x32_bf16 v[76:79], v[140:143], v[204:207], v[76:79]\nds_read_b64_tr_b16 v[110:111], v225 offset:0\n" \
  "buffer_load_dwordx4 v[188:191], %[vH], %[srdH], %[sH] offen offset:1024 sc1\ns_add_u32 %[sH], %[sH], 8192\ns_sub_u32 %[cnt], %[cnt], 1\ns_cmp_lg_u32 %[cnt], 0\n" \
  "s_cbranch_scc1 Lk%=\ns_waitcnt vmcnt(20)\ns_waitcnt lgkmcnt(8)\nv_mfma_f32_16x16x32_bf16 v[16:19], v[80:83], v[144:147], v[16:19]\nds_read_b64_tr_b16 v[112:113], v210 offset:8192\n" \
  "v_mfma_f32_16x16x32_bf16 v[48:51], v[80:83], v[148:151], v[48:51]\nds_read_b64_tr_b16 v[114:115], v211 offset:8192\ns_add_u32 m0, %[ldsw], 49152\n" \
  "v_mfma_f32_16x16x32_bf16 v[20:23], v[84:87], v[144:147], v[20:23]\nds_read_b64_tr_b16 v[116:117], v212 offset:8192\n" \
  "v_mfma_f32_16x16x32_bf16 v[52:55], v[84:87], v[148:151], v[52:55]\nds_read_b64_tr_b16 v[118:119], v213 offset:8192\nbuffer_load_dwordx4 v208, %[srdW], %[sW] offen offset:0 lds\n" \
  "v_mfma_f32_16x16x32_bf16 v[24:27], v[88:91], v[144:147], v[24:27]\nds_read_b64_tr_b16 v[120:121], v214 offset:8192\n" \
  "v_mfma_f32_16x16x32_bf16 v[56:59], v[88:91], v[148:151], v[56:59]\nds_read_b64_tr_b16 v[122:123], v215 offset:8192\n" \
  "v_mfma_f32_16x16x32_bf16 v[28:31], v[92:95], v[144:147], v[28:31]\nds_read_b64_tr_b16 v[124:125], v216 offset:8192\n" \
  "v_mfma_f32_16x16x32_bf16 v[60:63], v[92:95], v[148:151], v[60:63]\nds_read_b64_tr_b16 v[126:127], v217 offset:8192\nbuffer_load_dwordx4 v209, %[srdW], %[sW] offen offset:1024 lds\n" \
  "s_waitcnt lgkmcnt(8)\nv_mfma_f32_16x16x32_bf16 v[32:35], v[96:99], v[144:147], v[32:35]\nds_read_b64_tr_b16 v[128:129], v218 offset:8192\n" \
  "v_mfma_f32_16x16x32_bf16 v[64:67], v[96:99], v[148:151], v[64:67]\nds_read_b64_tr_b16 v[130:131], v219 offset:8192\ns_add_u32 %[sW], %[sW], %[incW]\n" \
  "v_mfma_f32_16x16x32_bf16 v[36:39], v[100:103], v[144:147], v[36:39]\nds_read_b64_tr_b16 v[132:133], v220 offset:8192\n" \
  "v_mfma_f32_16x16x32_bf16 v[68:71], v[100:103], v[148:151], v[68:71]\nds_read_b64_tr_b16 v[134:135], v221 offset:8192\n" \
  "buffer_load_dwordx4 v[192:195], %[vH], %[srdH], %[sH] offen offset:0 sc1\nv_mfma_f32_16x16x32_bf16 v[40:43], v[104:107], v[144:147], v[40:43]\n" \
  "ds_read_b64_tr_b16 v[136:137], v222 offset:8192\nv_mfma_f32_16x16x32_bf16 v[72:75], v[104:107], v[148:151], v[72:75]\nds_read_b64_tr_b16 v[138:139], v223 offset:8192\n" \
  "v_mfma_f32_16x16x32_bf16 v[44:47], v[108:111], v[144:147], v[44:47]\nds_read_b64_tr_b16 v[140:141], v224 offset:8192\n" \
  "v_mfma_f32_16x16x32_bf16 v[76:79], v[108:111], v[148:151], v[76:79]\nds_read_b64_tr_b16 v[142:143], v225 offset:8192\n" \
  "buffer_load_dwordx4 v[196:199], %[vH], %[srdH], %[sH] offen offset:1024 sc1\ns_add_u32 %[sH], %[sH], 8192\ns_waitcnt vmcnt(14)\ns_barrier\ns_waitcnt lgkmcnt(8)\n" \
  "v_mfma_f32_16x16x32_bf16 v[16:19], v[112:115], v[152:155], v[16:19]\nds_read_b64_tr_b16 v[80:81], v210 offset:16384\n" \
  "v_mfma_f32_16x16x32_bf16 v[48:51], v[112:115], v[156:159], v[48:51]\nds_read_b64_tr_b16 v[82:83], v211 offset:16384\ns_add_u32 m0, %[ldsw], 57344\n" \
  "v_mfma_f32_16x16x32_bf16 v[20:23], v[116:119], v[152:155], v[20:23]\nds_read_b64_tr_b16 v[84:85], v212 offset:16384\n" \
  "v_mfma_f32_16x16x32_bf16 v[52:55], v[116:119], v[156:159], v[52:55]\nds_read_b64_tr_b16 v[86:87], v213 offset:16384\nbuffer_load_dwordx4 v208, %[srdW], %[sW] offen offset:0 lds\n" \
  "v_mfma_f32_16x16x32_bf16 v[24:27], v[120:123], v[152:155], v[24:27]\nds_read_b64_tr_b16 v[88:89], v214 offset:16384\n" \
  "v_mfma_f32_16x16x32_bf16 v[56:59], v[120:123], v[156:159], v[56:59]\nds_read_b64_tr_b16 v[90:91], v215 offset:16384\n" \
  "v_mfma_f32_16x16x32_bf16 v[28:31], v[124:127], v[152:155], v[28:31]\nds_read_b64_tr_b16 v[92:93], v216 offset:16384\n" \
  "v_mfma_f32_16x16x32_bf16 v[60:63], v[124:127], v[156:159], v[60:63]\nds_read_b64_tr_b16 v[94:95], v217 offset:16384\n" \
  "buffer_load_dwordx4 v209, %[srdW], %[sW] offen offset:1024 lds\ns_waitcnt lgkmcnt(8)\nv_mfma_f32_16x16x32_bf16 v[32:35], v[128:131], v[152:155], v[32:35]\n" \
  "ds_read_b64_tr_b16 v[96:97], v218 offset:16384\nv_mfma_f32_16x16x32_bf16 v[64:67], v[128:131], v[156:159], v[64:67]\nds_read_b64_tr_b16 v[98:99], v219 offset:16384\n" \
  "s_add_u32 %[sW], %[sW], %[incW]\nv_mfma_f32_16x16x32_bf16 v[36:39], v[132:135], v[152:155], v[36:39]\nds_read_b64_tr_b16 v[100:101], v220 offset:16384\n" \
  "v_mfma_f32_16x16x32_bf16 v[68:71], v[132:135], v[156:159], v[68:71]\nds_read_b64_tr_b16 v[102:103], v221 offset:16384\n" \
  "buffer_load_dwordx4 v[200:203], %[vH], %[srdH], %[sH] offen offset:0 sc1\nv_mfma_f32_16x16x32_bf16 v[40:43], v[136:139], v[152:155], v[40:43]\n" \
  "ds_read_b64_tr_b16 v[104:105], v222 offset:16384\nv_mfma_f32_16x16x32_bf16 v[72:75], v[136:139], v[156:159], v[72:75]\nds_read_b64_tr_b16 v[106:107], v223 offset:16384\n" \
  "v_mfma_f32_16x16x32_bf16 v[44:47], v[140:143], v[152:155], v[44:47]\nds_read_b64_tr_b16 v[108:109], v224 offset:16384\n" \
  "v_mfma_f32_16x16x32_bf16 v[76:79], v[140:143], v[156:159], v[76:79]\nds_read_b64_tr_b16 v[110:111], v225 offset:16384\n" \
  "buffer_load_dwordx4 v[204:207], %[vH], %[srdH], %[sH] offen offset:1024 sc1\ns_add_u32 %[sH], %[sH], 8192\ns_waitcnt vmcnt(20)\ns_waitcnt lgkmcnt(8)\n" \
  "v_mfma_f32_16x16x32_bf16 v[16:19], v[80:83], v[160:163], v[16:19]\nds_read_b64_tr_b16 v[112:113], v210 offset:24576\n" \
  "v_mfma_f32_16x16x32_bf16 v[48:51], v[80:83], v[164:167], v[48:51]\nds_read_b64_tr_b16 v[114:115], v211 offset:24576\n" \
  "v_mfma_f32_16x16x32_bf16 v[20:23], v[84:87], v[160:163], v[20:23]\nds_read_b64_tr_b16 v[116:117], v212 offset:24576\n" \
  "v_mfma_f32_16x16x32_bf16 v[52:55], v[84:87], v[164:167], v[52:55]\nds_read_b64_tr_b16 v[118:119], v213 offset:24576\n" \
  "v_mfma_f32_16x16x32_bf16 v[24:27], v[88:91], v[160:163], v[24:27]\nds_read_b64_tr_b16 v[120:121], v214 offset:24576\n" \
  "v_mfma_f32_16x16x32_bf16 v[56:59], v[88:91], v[164:167], v[56:59]\nds_read_b64_tr_b16 v[122:123], v215 offset:24576\n" \
  "v_mfma_f32_16x16x32_bf16 v[28:31], v[92:95], v[160:163], v[28:31]\nds_read_b64_tr_b16 v[124:125], v216 offset:24576\n" \
  "v_mfma_f32_16x16x32_bf16 v[60:63], v[92:95], v[164:167], v[60:63]\nds_read_b64_tr_b16 v[126:127], v217 offset:24576\ns_waitcnt lgkmcnt(8)\n" \
  "v_mfma_f32_16x16x32_bf16 v[32:35], v[96:99], v[160:163], v[32:35]\nds_read_b64_tr_b16 v[128:129], v218 offset:24576\n" \
  "v_mfma_f32_16x16x32_bf16 v[64:67], v[96:99], v[164:167], v[64:67]\nds_read_b64_tr_b16 v[130:131], v219 offset:24576\n" \
  "v_mfma_f32_16x16x32_bf16 v[36:39], v[100:103], v[160:163], v[36:39]\nds_read_b64_tr_b16 v[132:133], v220 offset:24576\n" \
  "v_mfma_f32_16x16x32_bf16 v[68:71], v[100:103], v[164:167], v[68:71]\nds_read_b64_tr_b16 v[134:135], v221 offset:24576\n" \
  "v_mfma_f32_16x16x32_bf16 v[40:43], v[104:107], v[160:163], v[40:43]\nds_read_b64_tr_b16 v[136:137], v222 offset:24576\n" \
  "v_mfma_f32_16x16x32_bf16 v[72:75], v[104:107], v[164:167], v[72:75]\nds_read_b64_tr_b16 v[138:139], v223 offset:24576\n" \
  "v_mfma_f32_16x16x32_bf16 v[44:47], v[108:111], v[160:163], v[44:47]\nds_read_b64_tr_b16 v[140:141], v224 offset:24576\n" \
  "v_mfma_f32_16x16x32_bf16 v[76:79], v[108:111], v[164:167], v[76:79]\nds_read_b64_tr_b16 v[142:143], v225 offset:24576\ns_waitcnt vmcnt(10)\ns_barrier\ns_waitcnt lgkmcnt(8)\n" \
  "v_mfma_f32_16x16x32_bf16 v[16:19], v[112:115], v[168:171], v[16:19]\nds_read_b64_tr_b16 v[80:81], v210 offset:32768\n" \
  "v_mfma_f32_16x16x32_bf16 v[48:51], v[112:115], v[172:175], v[48:51]\nds_read_b64_tr_b16 v[82:83], v211 offset:32768\n" \
  "v_mfma_f32_16x16x32_bf16 v[20:23], v[116:119], v[168:171], v[20:23]\nds_read_b64_tr_b16 v[84:85], v212 offset:32768\n" \
  "v_mfma_f32_16x16x32_bf16 v[52:55], v[116:119], v[172:175], v[52:55]\nds_read_b64_tr_b16 v[86:87], v213 offset:32768\n" \
  "v_mfma_f32_16x16x32_bf16 v[24:27], v[120:123], v[168:171], v[24:27]\nds_read_b64_tr_b16 v[88:89], v214 offset:32768\n" \
  "v_mfma_f32_16x16x32_bf16 v[56:59], v[120:123], v[172:175], v[56:59]\nds_read_b64_tr_b16 v[90:91], v215 offset:32768\n" \
  "v_mfma_f32_16x16x32_bf16 v[28:31], v[124:127], v[168:171], v[28:31]\nds_read_b64_tr_b16 v[92:93], v216 offset:32768\n" \
  "v_mfma_f32_16x16x32_bf16 v[60:63], v[124:127], v[172:175], v[60:63]\nds_read_b64_tr_b16 v[94:95], v217 offset:32768\ns_waitcnt lgkmcnt(8)\n" \
  "v_mfma_f32_16x16x32_bf16 v[32:35], v[128:131], v[168:171], v[32:35]\nds_read_b64_tr_b16 v[96:97], v218 offset:32768\n" \
  "v_mfma_f32_16x16x32_bf16 v[64:67], v[128:131], v[172:175], v[64:67]\nds_read_b64_tr_b16 v[98:99], v219 offset:32768\n" \
  "v_mfma_f32_16x16x32_bf16 v[36:39], v[132:135], v[168:171], v[36:39]\nds_read_b64_tr_b16 v[100:101], v220 offset:32768\n" \
  "v_mfma_f32_16x16x32_bf16 v[68:71], v[132:135], v[172:175], v[68:71]\nds_read_b64_tr_b16 v[102:103], v221 offset:32768\n" \
  "v_mfma_f32_16x16x32_bf16 v[40:43], v[136:139], v[168:171], v[40:43]\nds_read_b64_tr_b16 v[104:105], v222 offset:32768\n" \
  "v_mfma_f32_16x16x32_bf16 v[72:75], v[136:139], v[172:175], v[72:75]\nds_read_b64_tr_b16 v[106:107], v223 offset:32768\n" \
  "v_mfma_f32_16x16x32_bf16 v[44:47], v[140:143], v[168:171], v[44:47]\nds_read_b64_tr_b16 v[108:109], v224 offset:32768\n" \
  "v_mfma_f32_16x16x32_bf16 v[76:79], v[140:143], v[172:175], v[76:79]\nds_read_b64_tr_b16 v[110:111], v225 offset:32768\ns_waitcnt vmcnt(12)\ns_waitcnt lgkmcnt(8)\n" \
  "v_mfma_f32_16x16x32_bf16 v[16:19], v[80:83], v[176:179], v[16:19]\nds_read_b64_tr_b16 v[112:113], v210 offset:40960\n" \
  "v_mfma_f32_16x16x32_bf16 v[48:51], v[80:83], v[180:183], v[48:51]\nds_read_b64_tr_b16 v[114:115], v211 offset:40960\n" \
  "v_mfma_f32_16x16x32_bf16 v[20:23], v[84:87], v[176:179], v[20:23]\nds_read_b64_tr_b16 v[116:117], v212 offset:40960\n" \
  "v_mfma_f32_16x16x32_bf16 v[52:55], v[84:87], v[180:183], v[52:55]\nds_read_b64_tr_b16 v[118:119], v213 offset:40960\n" \
  "v_mfma_f32_16x16x32_bf16 v[24:27], v[88:91], v[176:179], v[24:27]\nds_read_b64_tr_b16 v[120:121], v214 offset:40960\n" \
  "v_mfma_f32_16x16x32_bf16 v[56:59], v[88:91], v[180:183], v[56:59]\nds_read_b64_tr_b16 v[122:123], v215 offset:40960\n" \
  "v_mfma_f32_16x16x32_bf16 v[28:31], v[92:95], v[176:179], v[28:31]\nds_read_b64_tr_b16 v[124:125], v216 offset:40960\n" \
  "v_mfma_f32_16x16x32_bf16 v[60:63], v[92:95], v[180:183], v[60:63]\nds_read_b64_tr_b16 v[126:127], v217 offset:40960\ns_waitcnt lgkmcnt(8)\n" \
  "v_mfma_f32_16x16x32_bf16 v[32:35], v[96:99], v[176:179], v[32:35]\nds_read_b64_tr_b16 v[128:129], v218 offset:40960\n" \
  "v_mfma_f32_16x16x32_bf16 v[64:67], v[96:99], v[180:183], v[64:67]\nds_read_b64_tr_b16 v[130:131], v219 offset:40960\n" \
  "v_mfma_f32_16x16x32_bf16 v[36:39], v[100:103], v[176:179], v[36:39]\nds_read_b64_tr_b16 v[132:133], v220 offset:40960\n" \
  "v_mfma_f32_16x16x32_bf16 v[68:71], v[100:103], v[180:183], v[68:71]\nds_read_b64_tr_b16 v[134:135], v221 offset:40960\n" \
  "v_mfma_f32_16x16x32_bf16 v[40:43], v[104:107], v[176:179], v[40:43]\nds_read_b64_tr_b16 v[136:137], v222 offset:40960\n" \
  "v_mfma_f32_16x16x32_bf16 v[72:75], v[104:107], v[180:183], v[72:75]\nds_read_b64_tr_b16 v[138:139], v223 offset:40960\n" \
  "v_mfma_f32_16x16x32_bf16 v[44:47], v[108:111], v[176:179], v[44:47]\nds_read_b64_tr_b16 v[140:141], v224 offset:40960\n" \
  "v_mfma_f32_16x16x32_bf16 v[76:79], v[108:111], v[180:183], v[76:79]\nds_read_b64_tr_b16 v[142:143], v225 offset:40960\ns_waitcnt vmcnt(2)\ns_barrier\ns_waitcnt lgkmcnt(8)\n" \
  "v_mfma_f32_16x16x32_bf16 v[16:19], v[112:115], v[184:187], v[16:19]\nds_read_b64_tr_b16 v[80:81], v210 offset:49152\n" \
  "v_mfma_f32_16x16x32_bf16 v[48:51], v[112:115], v[188:191], v[48:51]\nds_read_b64_tr_b16 v[82:83], v211 offset:49152\n" \
  "v_mfma_f32_16x16x32_bf16 v[20:23], v[116:119], v[184:187], v[20:23]\nds_read_b64_tr_b16 v[84:85], v212 offset:49152\n" \
  "v_mfma_f32_16x16x32_bf16 v[52:55], v[116:119], v[188:191], v[52:55]\nds_read_b64_tr_b16 v[86:87], v213 offset:49152\n" \
  "v_mfma_f32_16x16x32_bf16 v[24:27], v[120:123], v[184:187], v[24:27]\nds_read_b64_tr_b16 v[88:89], v214 offset:49152\n" \
  "v_mfma_f32_16x16x32_bf16 v[56:59], v[120:123], v[188:191], v[56:59]\nds_read_b64_tr_b16 v[90:91], v215 offset:49152\n" \
  "v_mfma_f32_16x16x32_bf16 v[28:31], v[124:127], v[184:187], v[28:31]\nds_read_b64_tr_b16 v[92:93], v216 offset:49152\n" \
  "v_mfma_f32_16x16x32_bf16 v[60:63], v[124:127], v[188:191], v[60:63]\nds_read_b64_tr_b16 v[94:95], v217 offset:49152\ns_waitcnt lgkmcnt(8)\n" \
  "v_mfma_f32_16x16x32_bf16 v[32:35], v[128:131], v[184:187], v[32:35]\nds_read_b64_tr_b16 v[96:97], v218 offset:49152\n" \
  "v_mfma_f32_16x16x32_bf16 v[64:67], v[128:131], v[188:191], v[64:67]\nds_read_b64_tr_b16 v[98:99], v219 offset:49152\n" \
  "v_mfma_f32_16x16x32_bf16 v[36:39], v[132:135], v[184:187], v[36:39]\nds_read_b64_tr_b16 v[100:101], v220 offset:49152\n" \
  "v_mfma_f32_16x16x32_bf16 v[68:71], v[132:135], v[188:191], v[68:71]\nds_read_b64_tr_b16 v[102:103], v221 offset:49152\n" \
  "v_mfma_f32_16x16x32_bf16 v[40:43], v[136:139], v[184:187], v[40:43]\nds_read_b64_tr_b16 v[104:105], v222 offset:49152\n" \
  "v_mfma_f32_16x16x32_bf16 v[72:75], v[136:139], v[188:191], v[72:75]\nds_read_b64_tr_b16 v[106:107], v223 offset:49152\n" \
  "v_mfma_f32_16x16x32_bf16 v[44:47], v[140:143], v[184:187], v[44:47]\nds_read_b64_tr_b16 v[108:109], v224 offset:49152\n" \
  "v_mfma_f32_16x16x32_bf16 v[76:79], v[140:143], v[188:191], v[76:79]\nds_read_b64_tr_b16 v[110:111], v225 offset:49152\ns_waitcnt vmcnt(4)\ns_waitcnt lgkmcnt(8)\n" \
  "v_mfma_f32_16x16x32_bf16 v[16:19], v[80:83], v[192:195], v[16:19]\nds_read_b64_tr_b16 v[112:113], v210 offset:57344\n" \
  "v_mfma_f32_16x16x32_bf16 v[48:51], v[80:83], v[196:199], v[48:51]\nds_read_b64_tr_b16 v[114:115], v211 offset:57344\n" \
  "v_mfma_f32_16x16x32_bf16 v[20:23], v[84:87], v[192:195], v[20:23]\nds_read_b64_tr_b16 v[116:117], v212 offset:57344\n" \
  "v_mfma_f32_16x16x32_bf16 v[52:55], v[84:87], v[196:199], v[52:55]\nds_read_b64_tr_b16 v[118:119], v213 offset:57344\n" \
  "v_mfma_f32_16x16x32_bf16 v[24:27], v[88:91], v[192:195], v[24:27]\nds_read_b64_tr_b16 v[120:121], v214 offset:57344\n" \
  "v_mfma_f32_16x16x32_bf16 v[56:59], v[88:91], v[196:199], v[56:59]\nds_read_b64_tr_b16 v[122:123], v215 offset:57344\n" \
  "v_mfma_f32_16x16x32_bf16 v[28:31], v[92:95], v[192:195], v[28:31]\nds_read_b64_tr_b16 v[124:125], v216 offset:57344\n" \
  "v_mfma_f32_16x16x32_bf16 v[60:63], v[92:95], v[196:199], v[60:63]\nds_read_b64_tr_b16 v[126:127], v217 offset:57344\ns_waitcnt lgkmcnt(8)\n" \
  "v_mfma_f32_16x16x32_bf16 v[32:35], v[96:99], v[192:195], v[32:35]\nds_read_b64_tr_b16 v[128:129], v218 offset:57344\n" \
  "v_mfma_f32_16x16x32_bf16 v[64:67], v[96:99], v[196:199], v[64:67]\nds_read_b64_tr_b16 v[130:131], v219 offset:57344\n" \
  "v_mfma_f32_16x16x32_bf16 v[36:39], v[100:103], v[192:195], v[36:39]\nds_read_b64_tr_b16 v[132:133], v220 offset:57344\n" \
  "v_mfma_f32_16x16x32_bf16 v[68:71], v[100:103], v[196:199], v[68:71]\nds_read_b64_tr_b16 v[134:135], v221 offset:57344\n" \
  "v_mfma_f32_16x16x32_bf16 v[40:43], v[104:107], v[192:195], v[40:43]\nds_read_b64_tr_b16 v[136:137], v222 offset:57344\n" \
  "v_mfma_f32_16x16x32_bf16 v[72:75], v[104:107], v[196:199], v[72:75]\nds_read_b64_tr_b16 v[138:139], v223 offset:57344\n" \
  "v_mfma_f32_16x16x32_bf16 v[44:47], v[108:111], v[192:195], v[44:47]\nds_read_b64_tr_b16 v[140:141], v224 offset:57344\n" \
  "v_mfma_f32_16x16x32_bf16 v[76:79], v[108:111], v[196:199], v[76:79]\nds_read_b64_tr_b16 v[142:143], v225 offset:57344\ns_waitcnt vmcnt(0)\ns_waitcnt lgkmcnt(8)\n" \
  "v_mfma_f32_16x16x32_bf16 v[16:19], v[112:115], v[200:203], v[16:19]\nv_mfma_f32_16x16x32_bf16 v[48:51], v[112:115], v[204:207], v[48:51]\n" \
  "v_mfma_f32_16x16x32_bf16 v[20:23], v[116:119], v[200:203], v[20:23]\nv_mfma_f32_16x16x32_bf16 v[52:55], v[116:119], v[204:207], v[52:55]\n" \
  "v_mfma_f32_16x16x32_bf16 v[24:27], v[120:123], v[200:203], v[24:27]\nv_mfma_f32_16x16x32_bf16 v[56:59], v[120:123], v[204:207], v[56:59]\n" \
  "v_mfma_f32_16x16x32_bf16 v[28:31], v[124:127], v[200:203], v[28:31]\nv_mfma_f32_16x16x32_bf16 v[60:63], v[124:127], v[204:207], v[60:63]\ns_waitcnt lgkmcnt(0)\n" \
  "v_mfma_f32_16x16x32_bf16 v[32:35], v[128:131], v[200:203], v[32:35]\nv_mfma_f32_16x16x32_bf16 v[64:67], v[128:131], v[204:207], v[64:67]\n" \
  "v_mfma_f32_16x16x32_bf16 v[36:39], v[132:135], v[200:203], v[36:39]\nv_mfma_f32_16x16x32_bf16 v[68:71], v[132:135], v[204:207], v[68:71]\n" \
  "v_mfma_f32_16x16x32_bf16 v[40:43], v[136:139], v[200:203], v[40:43]\nv_mfma_f32_16x16x32_bf16 v[72:75], v[136:139], v[204:207], v[72:75]\n" \
  "v_mfma_f32_16x16x32_bf16 v[44:47], v[140:143], v[200:203], v[44:47]\nv_mfma_f32_16x16x32_bf16 v[76:79], v[140:143], v[204:207], v[76:79]\ns_nop 7\ns_nop 1\n"
#define GEMM2_PROW \
  "s_nop 4\ns_add_u32 m0, %[ldsw], 0\ns_nop 0\nbuffer_load_dwordx4 v208, %[srdW], %[sW] offen offset:0 lds\nbuffer_load_dwordx4 v209, %[srdW], %[sW] offen offset:1024 lds\n" \
  "s_add_u32 %[sW], %[sW], %[incW]\ns_add_u32 m0, %[ldsw], 8192\ns_nop 0\nbuffer_load_dwordx4 v208, %[srdW], %[sW] offen offset:0 lds\n" \
  "buffer_load_dwordx4 v209, %[srdW], %[sW] offen offset:1024 lds\ns_add_u32 %[sW], %[sW], %[incW]\ns_add_u32 m0, %[ldsw], 16384\ns_nop 0\n" \
  "buffer_load_dwordx4 v208, %[srdW], %[sW] offen offset:0 lds\nbuffer_load_dwordx4 v209, %[srdW], %[sW] offen offset:1024 lds\ns_add_u32 %[sW], %[sW], %[incW]\n"
#define GEMM2_PROW2 \
  "s_nop 4\ns_add_u32 m0, %[ldsw], 24576\ns_nop 0\nbuffer_load_dwordx4 v208, %[srdW], %[sW] offen offset:0 lds\nbuffer_load_dwordx4 v209, %[srdW], %[sW] offen offset:1024 lds\n" \
  "s_add_u32 %[sW], %[sW], %[incW]\ns_add_u32 m0, %[ldsw], 32768\ns_nop 0\nbuffer_load_dwordx4 v208, %[srdW], %[sW] offen offset:0 lds\n" \
  "buffer_load_dwordx4 v209, %[srdW], %[sW] offen offset:1024 lds\ns_add_u32 %[sW], %[sW], %[incW]\ns_add_u32 m0, %[ldsw], 40960\ns_nop 0\n" \
  "buffer_load_dwordx4 v208, %[srdW], %[sW] offen offset:0 lds\nbuffer_load_dwordx4 v209, %[srdW], %[sW] offen offset:1024 lds\ns_add_u32 %[sW], %[sW], %[incW]\n"
#define GEMM2_PRO \
  "s_nop 4\nbuffer_load_dword v234, %[vN], %[srdH], 0 offen\nbuffer_load_dword v234, %[vN], %[srdH], 0 offen\n" \
  "buffer_load_dwordx4 v[144:147], %[vH], %[srdH], %[sH] offen offset:0 sc1\nbuffer_load_dwordx4 v[148:151], %[vH], %[srdH], %[sH] offen offset:1024 sc1\n" \
  "s_add_u32 %[sH], %[sH], 8192\nbuffer_load_dword v234, %[vN], %[srdH], 0 offen\nbuffer_load_dword v234, %[vN], %[srdH], 0 offen\n" \
  "buffer_load_dwordx4 v[152:155], %[vH], %[srdH], %[sH] offen offset:0 sc1\nbuffer_load_dwordx4 v[156:159], %[vH], %[srdH], %[sH] offen offset:1024 sc1\n" \
  "s_add_u32 %[sH], %[sH], 8192\nbuffer_load_dword v234, %[vN], %[srdH], 0 offen\nbuffer_load_dword v234, %[vN], %[srdH], 0 offen\n" \
  "buffer_load_dwordx4 v[160:163], %[vH], %[srdH], %[sH] offen offset:0 sc1\nbuffer_load_dwordx4 v[164:167], %[vH], %[srdH], %[sH] offen offset:1024 sc1\n" \
  "s_add_u32 %[sH], %[sH], 8192\nbuffer_load_dword v234, %[vN], %[srdH], 0 offen\nbuffer_load_dword v234, %[vN], %[srdH], 0 offen\n" \
  "buffer_load_dwordx4 v[168:171], %[vH], %[srdH], %[sH] offen offset:0 sc1\nbuffer_load_dwordx4 v[172:175], %[vH], %[srdH], %[sH] offen offset:1024 sc1\n" \
  "s_add_u32 %[sH], %[sH], 8192\nbuffer_load_dword v234, %[vN], %[srdH], 0 offen\nbuffer_load_dword v234, %[vN], %[srdH], 0 offen\n" \
  "buffer_load_dwordx4 v[176:179], %[vH], %[srdH], %[sH] offen offset:0 sc1\nbuffer_load_dwordx4 v[180:183], %[vH], %[srdH], %[sH] offen offset:1024 sc1\n" \
  "s_add_u32 %[sH], %[sH], 8192\nbuffer_load_dword v234, %[vN], %[srdH], 0 offen\nbuffer_load_dword v234, %[vN], %[srdH], 0 offen\n" \
  "buffer_load_dwordx4 v[184:187], %[vH], %[srdH], %[sH] offen offset:0 sc1\nbuffer_load_dwordx4 v[188:191], %[vH], %[srdH], %[sH] offen offset:1024 sc1\n" \
  "s_add_u32 %[sH], %[sH], 8192\n"
#define GEMM2_ASM \
  "s_waitcnt vmcnt(24) lgkmcnt(0)\ns_barrier\nds_read_b64_tr_b16 v[80:81], v210 offset:0\nds_read_b64_tr_b16 v[82:83], v211 offset:0\nds_read_b64_tr_b16 v[84:85], v212 offset:0\n" \
  "ds_read_b64_tr_b16 v[86:87], v213 offset:0\nds_read_b64_tr_b16 v[88:89], v214 offset:0\nds_read_b64_tr_b16 v[90:91], v215 offset:0\nds_read_b64_tr_b16 v[92:93], v216 offset:0\n" \
  "ds_read_b64_tr_b16 v[94:95], v217 offset:0\nds_read_b64_tr_b16 v[96:97], v218 offset:0\nds_read_b64_tr_b16 v[98:99], v219 offset:0\nds_read_b64_tr_b16 v[100:101], v220 offset:0\n" \
  "ds_read_b64_tr_b16 v[102:103], v221 offset:0\nds_read_b64_tr_b16 v[104:105], v222 offset:0\nds_read_b64_tr_b16 v[106:107], v223 offset:0\n" \
  "ds_read_b64_tr_b16 v[108:109], v224 offset:0\nds_read_b64_tr_b16 v[110:111], v225 offset:0\nv_mov_b32 v16, 0\nv_mov_b32 v17, 0\nv_mov_b32 v18, 0\nv_mov_b32 v19, 0\n" \
  "v_mov_b32 v20, 0\nv_mov_b32 v21, 0\nv_mov_b32 v22, 0\nv_mov_b32 v23, 0\nv_mov_b32 v24, 0\nv_mov_b32 v25, 0\nv_mov_b32 v26, 0\nv_mov_b32 v27, 0\nv_mov_b32 v28, 0\nv_mov_b32 v29, 0\n" \
  "v_mov_b32 v30, 0\nv_mov_b32 v31, 0\nv_mov_b32 v32, 0\nv_mov_b32 v33, 0\nv_mov_b32 v34, 0\nv_mov_b32 v35, 0\nv_mov_b32 v36, 0\nv_mov_b32 v37, 0\nv_mov_b32 v38, 0\nv_mov_b32 v39, 0\n" \
  "v_mov_b32 v40, 0\nv_mov_b32 v41, 0\nv_mov_b32 v42, 0\nv_mov_b32 v43, 0\nv_mov_b32 v44, 0\nv_mov_b32 v45, 0\nv_mov_b32 v46, 0\nv_mov_b32 v47, 0\nv_mov_b32 v48, 0\nv_mov_b32 v49, 0\n" \
  "v_mov_b32 v50, 0\nv_mov_b32 v51, 0\nv_mov_b32 v52, 0\nv_mov_b32 v53, 0\nv_mov_b32 v54, 0\nv_mov_b32 v55, 0\nv_mov_b32 v56, 0\nv_mov_b32 v57, 0\nv_mov_b32 v58, 0\nv_mov_b32 v59, 0\n" \
  "v_mov_b32 v60, 0\nv_mov_b32 v61, 0\nv_mov_b32 v62, 0\nv_mov_b32 v63, 0\nv_mov_b32 v64, 0\nv_mov_b32 v65, 0\nv_mov_b32 v66, 0\nv_mov_b32 v67, 0\nv_mov_b32 v68, 0\nv_mov_b32 v69, 0\n" \
  "v_mov_b32 v70, 0\nv_mov_b32 v71, 0\nv_mov_b32 v72, 0\nv_mov_b32 v73, 0\nv_mov_b32 v74, 0\nv_mov_b32 v75, 0\nv_mov_b32 v76, 0\nv_mov_b32 v77, 0\nv_mov_b32 v78, 0\nv_mov_b32 v79, 0\n" \
  "s_mov_b32 %[cnt], 6\nLk%=:\ns_waitcnt vmcnt(20)\ns_waitcnt lgkmcnt(8)\nv_mfma_f32_16x16x32_bf16 v[16:19], v[80:83], v[144:147], v[16:19]\n" \
  "ds_read_b64_tr_b16 v[112:113], v210 offset:8192\nv_mfma_f32_16x16x32_bf16 v[48:51], v[80:83], v[148:151], v[48:51]\nds_read_b64_tr_b16 v[114:115], v211 offset:8192\n" \
  "s_add_u32 m0, %[ldsw], 49152\nv_mfma_f32_16x16x32_bf16 v[20:23], v[84:87], v[144:147], v[20:23]\nds_read_b64_tr_b16 v[116:117], v212 offset:8192\n" \
  "v_mfma_f32_16x16x32_bf16 v[52:55], v[84:87], v[148:151], v[52:55]\nds_read_b64_tr_b16 v[118:119], v213 offset:8192\nbuffer_load_dwordx4 v208, %[srdW], %[sW] offen offset:0 lds\n" \
  "v_mfma_f32_16x16x32_bf16 v[24:27], v[88:91], v[144:147], v[24:27]\nds_read_b64_tr_b16 v[120:121], v214 offset:8192\ns_cmp_eq_u32 %[cnt], %[jcnt]\n" \
  "v_mfma_f32_16x16x32_bf16 v[56:59], v[88:91], v[148:151], v[56:59]\nds_read_b64_tr_b16 v[122:123], v215 offset:8192\ns_cselect_b32 %[incX], %[jmp], %[incW]\n" \
  "s_cselect_b32 %[incY], %[jmpH], 8192\nv_mfma_f32_16x16x32_bf16 v[28:31], v[92:95], v[144:147], v[28:31]\nds_read_b64_tr_b16 v[124:125], v216 offset:8192\n" \
  "s_cmp_eq_u32 %[cnt], %[tcnt]\ns_cbranch_scc0 Lt%=\nbuffer_load_dword v234, %[vT], %[srdW], 0 offen\nLt%=:\nv_mfma_f32_16x16x32_bf16 v[60:63], v[92:95], v[148:151], v[60:63]\n" \
  "ds_read_b64_tr_b16 v[126:127], v217 offset:8192\nbuffer_load_dwordx4 v209, %[srdW], %[sW] offen offset:1024 lds\ns_waitcnt lgkmcnt(8)\n" \
  "v_mfma_f32_16x16x32_bf16 v[32:35], v[96:99], v[144:147], v[32:35]\nds_read_b64_tr_b16 v[128:129], v218 offset:8192\n" \
  "v_mfma_f32_16x16x32_bf16 v[64:67], v[96:99], v[148:151], v[64:67]\nds_read_b64_tr_b16 v[130:131], v219 offset:8192\ns_add_u32 %[sW], %[sW], %[incW]\n" \
  "v_mfma_f32_16x16x32_bf16 v[36:39], v[100:103], v[144:147], v[36:39]\nds_read_b64_tr_b16 v[132:133], v220 offset:8192\n" \
  "v_mfma_f32_16x16x32_bf16 v[68:71], v[100:103], v[148:151], v[68:71]\nds_read_b64_tr_b16 v[134:135], v221 offset:8192\n" \
  "buffer_load_dwordx4 v[192:195], %[vH], %[srdH], %[sH] offen offset:0 sc1\nv_mfma_f32_16x16x32_bf16 v[40:43], v[104:107], v[144:147], v[40:43]\n" \
  "ds_read_b64_tr_b16 v[136:137], v222 offset:8192\nv_mfma_f32_16x16x32_bf16 v[72:75], v[104:107], v[148:151], v[72:75]\nds_read_b64_tr_b16 v[138:139], v223 offset:8192\n" \
  "v_mfma_f32_16x16x32_bf16 v[44:47], v[108:111], v[144:147], v[44:47]\nds_read_b64_tr_b16 v[140:141], v224 offset:8192\n" \
  "v_mfma_f32_16x16x32_bf16 v[76:79], v[108:111], v[148:151], v[76:79]\nds_read_b64_tr_b16 v[142:143], v225 offset:8192\n" \
  "buffer_load_dwordx4 v[196:199], %[vH], %[srdH], %[sH] offen offset:1024 sc1\ns_add_u32 %[sH], %[sH], 8192\ns_waitcnt vmcnt(14)\ns_barrier\ns_waitcnt lgkmcnt(8)\n" \
  "v_mfma_f32_16x16x32_bf16 v[16:19], v[112:115], v[152:155], v[16:19]\nds_read_b64_tr_b16 v[80:81], v210 offset:16384\n" \
  "v_mfma_f32_16x16x32_bf16 v[48:51], v[112:115], v[156:159], v[48:51]\nds_read_b64_tr_b16 v[82:83], v211 offset:16384\ns_add_u32 m0, %[ldsw], 57344\n" \
  "v_mfma_f32_16x16x32_bf16 v[20:23], v[116:119], v[152:155], v[20:23]\nds_read_b64_tr_b16 v[84:85], v212 offset:16384\n" \
  "v_mfma_f32_16x16x32_bf16 v[52:55], v[116:119], v[156:159], v[52:55]\nds_read_b64_tr_b16 v[86:87], v213 offset:16384\nbuffer_load_dwordx4 v208, %[srdW], %[sW] offen offset:0 lds\n" \
  "v_mfma_f32_16x16x32_bf16 v[24:27], v[120:123], v[152:155], v[24:27]\nds_read_b64_tr_b16 v[88:89], v214 offset:16384\n" \
  "v_mfma_f32_16x16x32_bf16 v[56:59], v[120:123], v[156:159], v[56:59]\nds_read_b64_tr_b16 v[90:91], v215 offset:16384\n" \
  "v_mfma_f32_16x16x32_bf16 v[28:31], v[124:127], v[152:155], v[28:31]\nds_read_b64_tr_b16 v[92:93], v216 offset:16384\n" \
  "v_mfma_f32_16x16x32_bf16 v[60:63], v[124:127], v[156:159], v[60:63]\nds_read_b64_tr_b16 v[94:95], v217 offset:16384\n" \
  "buffer_load_dwordx4 v209, %[srdW], %[sW] offen offset:1024 lds\ns_waitcnt lgkmcnt(8)\nv_mfma_f32_16x16x32_bf16 v[32:35], v[128:131], v[152:155], v[32:35]\n" \
  "ds_read_b64_tr_b16 v[96:97], v218 offset:16384\nv_mfma_f32_16x16x32_bf16 v[64:67], v[128:131], v[156:159], v[64:67]\nds_read_b64_tr_b16 v[98:99], v219 offset:16384\n" \
  "s_add_u32 %[sW], %[sW], %[incX]\nv_mfma_f32_16x16x32_bf16 v[36:39], v[132:135], v[152:155], v[36:39]\nds_read_b64_tr_b16 v[100:101], v220 offset:16384\n" \
  "v_mfma_f32_16x16x32_bf16 v[68:71], v[132:135], v[156:159], v[68:71]\nds_read_b64_tr_b16 v[102:103], v221 offset:16384\n" \
  "buffer_load_dwordx4 v[200:203], %[vH], %[srdH], %[sH] offen offset:0 sc1\nv_mfma_f32_16x16x32_bf16 v[40:43], v[136:139], v[152:155], v[40:43]\n" \
  "ds_read_b64_tr_b16 v[104:105], v222 offset:16384\nv_mfma_f32_16x16x32_bf16 v[72:75], v[136:139], v[156:159], v[72:75]\nds_read_b64_tr_b16 v[106:107], v223 offset:16384\n" \
  "v_mfma_f32_16x16x32_bf16 v[44:47], v[140:143], v[152:155], v[44:47]\nds_read_b64_tr_b16 v[108:109], v224 offset:16384\n" \
  "v_mfma_f32_16x16x32_bf16 v[76:79], v[140:143], v[156:159], v[76:79]\nds_read_b64_tr_b16 v[110:111], v225 offset:16384\n" \
  "buffer_load_dwordx4 v[204:207], %[vH], %[srdH], %[sH] offen offset:1024 sc1\ns_add_u32 %[sH], %[sH], %[incY]\ns_waitcnt vmcnt(20)\ns_waitcnt lgkmcnt(8)\n" \
  "v_mfma_f32_16x16x32_bf16 v[16:19], v[80:83], v[160:163], v[16:19]\nds_read_b64_tr_b16 v[112:113], v210 offset:24576\n" \
  "v_mfma_f32_16x16x32_bf16 v[48:51], v[80:83], v[164:167], v[48:51]\nds_read_b64_tr_b16 v[114:115], v211 offset:24576\ns_add_u32 m0, %[ldsw], 0\n" \
  "v_mfma_f32_16x16x32_bf16 v[20:23], v[84:87], v[160:163], v[20:23]\nds_read_b64_tr_b16 v[116:117], v212 offset:24576\n" \
  "v_mfma_f32_16x16x32_bf16 v[52:55], v[84:87], v[164:167], v[52:55]\nds_read_b64_tr_b16 v[118:119], v213 offset:24576\nbuffer_load_dwordx4 v208, %[srdW], %[sW] offen offset:0 lds\n" \
  "v_mfma_f32_16x16x32_bf16 v[24:27], v[88:91], v[160:163], v[24:27]\nds_read_b64_tr_b16 v[120:121], v214 offset:24576\n" \
  "v_mfma_f32_16x16x32_bf16 v[56:59], v[88:91], v[164:167], v[56:59]\nds_read_b64_tr_b16 v[122:123], v215 offset:24576\n" \
  "v_mfma_f32_16x16x32_bf16 v[28:31], v[92:95], v[160:163], v[28:31]\nds_read_b64_tr_b16 v[124:125], v216 offset:24576\n" \
  "v_mfma_f32_16x16x32_bf16 v[60:63], v[92:95], v[164:167], v[60:63]\nds_read_b64_tr_b16 v[126:127], v217 offset:24576\n" \
  "buffer_load_dwordx4 v209, %[srdW], %[sW] offen offset:1024 lds\ns_waitcnt lgkmcnt(8)\nv_mfma_f32_16x16x32_bf16 v[32:35], v[96:99], v[160:163], v[32:35]\n" \
  "ds_read_b64_tr_b16 v[128:129], v218 offset:24576\nv_mfma_f32_16x16x32_bf16 v[64:67], v[96:99], v[164:167], v[64:67]\nds_read_b64_tr_b16 v[130:131], v219 offset:24576\n" \
  "s_add_u32 %[sW], %[sW], %[incW]\nv_mfma_f32_16x16x32_bf16 v[36:39], v[100:103], v[160:163], v[36:39]\nds_read_b64_tr_b16 v[132:133], v220 offset:24576\n" \
  "v_mfma_f32_16x16x32_bf16 v[68:71], v[100:103], v[164:167], v[68:71]\nds_read_b64_tr_b16 v[134:135], v221 offset:24576\n" \
  "buffer_load_dwordx4 v[144:147], %[vH], %[srdH], %[sH] offen offset:0 sc1\nv_mfma_f32_16x16x32_bf16 v[40:43], v[104:107], v[160:163], v[40:43]\n" \
  "ds_read_b64_tr_b16 v[136:137], v222 offset:24576\nv_mfma_f32_16x16x32_bf16 v[72:75], v[104:107], v[164:167], v[72:75]\nds_read_b64_tr_b16 v[138:139], v223 offset:24576\n" \
  "v_mfma_f32_16x16x32_bf16 v[44:47], v[108:111], v[160:163], v[44:47]\nds_read_b64_tr_b16 v[140:141], v224 offset:24576\n" \
  "v_mfma_f32_16x16x32_bf16 v[76:79], v[108:111], v[164:167], v[76:79]\nds_read_b64_tr_b16 v[142:143], v225 offset:24576\n" \
  "buffer_load_dwordx4 v[148:151], %[vH], %[srdH], %[sH] offen offset:1024 sc1\ns_add_u32 %[sH], %[sH], 8192\ns_waitcnt vmcnt(14)\ns_barrier\ns_waitcnt lgkmcnt(8)\n" \
  "v_mfma_f32_16x16x32_bf16 v[16:19], v[112:115], v[168:171], v[16:19]\nds_read_b64_tr_b16 v[80:81], v210 offset:32768\n" \
  "v_mfma_f32_16x16x32_bf16 v[48:51], v[112:115], v[172:175], v[48:51]\nds_read_b64_tr_b16 v[82:83], v211 offset:32768\ns_add_u32 m0, %[ldsw], 8192\n" \
  "v_mfma_f32_16x16x32_bf16 v[20:23], v[116:119], v[168:171], v[20:23]\nds_read_b64_tr_b16 v[84:85], v212 offset:32768\n" \
  "v_mfma_f32_16x16x32_bf16 v[52:55], v[116:119], v[172:175], v[52:55]\nds_read_b64_tr_b16 v[86:87], v213 offset:32768\nbuffer_load_dwordx4 v208, %[srdW], %[sW] offen offset:0 lds\n" \
  "v_mfma_f32_16x16x32_bf16 v[24:27], v[120:123], v[168:171], v[24:27]\nds_read_b64_tr_b16 v[88:89], v214 offset:32768\n" \
  "v_mfma_f32_16x16x32_bf16 v[56:59], v[120:123], v[172:175], v[56:59]\nds_read_b64_tr_b16 v[90:91], v215 offset:32768\n" \
  "v_mfma_f32_16x16x32_bf16 v[28:31], v[124:127], v[168:171], v[28:31]\nds_read_b64_tr_b16 v[92:93], v216 offset:32768\n" \
  "v_mfma_f32_16x16x32_bf16 v[60:63], v[124:127], v[172:175], v[60:63]\nds_read_b64_tr_b16 v[94:95], v217 offset:32768\n" \
  "buffer_load_dwordx4 v209, %[srdW], %[sW] offen offset:1024 lds\ns_waitcnt lgkmcnt(8)\nv_mfma_f32_16x16x32_bf16 v[32:35], v[128:131], v[168:171], v[32:35]\n" \
  "ds_read_b64_tr_b16 v[96:97], v218 offset:32768\nv_mfma_f32_16x16x32_bf16 v[64:67], v[128:131], v[172:175], v[64:67]\nds_read_b64_tr_b16 v[98:99], v219 offset:32768\n" \
  "s_add_u32 %[sW], %[sW], %[incW]\nv_mfma_f32_16x16x32_bf16 v[36:39], v[132:135], v[168:171], v[36:39]\nds_read_b64_tr_b16 v[100:101], v220 offset:32768\n" \
  "v_mfma_f32_16x16x32_bf16 v[68:71], v[132:135], v[172:175], v[68:71]\nds_read_b64_tr_b16 v[102:103], v221 offset:32768\n" \
  "buffer_load_dwordx4 v[152:155], %[vH], %[srdH], %[sH] offen offset:0 sc1\nv_mfma_f32_16x16x32_bf16 v[40:43], v[136:139], v[168:171], v[40:43]\n" \
  "ds_read_b64_tr_b16 v[104:105], v222 offset:32768\nv_mfma_f32_16x16x32_bf16 v[72:75], v[136:139], v[172:175], v[72:75]\nds_read_b64_tr_b16 v[106:107], v223 offset:32768\n" \
  "v_mfma_f32_16x16x32_bf16 v[44:47], v[140:143], v[168:171], v[44:47]\nds_read_b64_tr_b16 v[108:109], v224 offset:32768\n" \
  "v_mfma_f32_16x16x32_bf16 v[76:79], v[140:143], v[172:175], v[76:79]\nds_read_b64_tr_b16 v[110:111], v225 offset:32768\n" \
  "buffer_load_dwordx4 v[156:159], %[vH], %[srdH], %[sH] offen offset:1024 sc1\ns_add_u32 %[sH], %[sH], 8192\ns_waitcnt vmcnt(20)\ns_waitcnt lgkmcnt(8)\n" \
  "v_mfma_f32_16x16x32_bf16 v[16:19], v[80:83], v[176:179], v[16:19]\nds_read_b64_tr_b16 v[112:113], v210 offset:40960\n" \
  "v_mfma_f32_16x16x32_bf16 v[48:51], v[80:83], v[180:183], v[48:51]\nds_read_b64_tr_b16 v[114:115], v211 offset:40960\ns_add_u32 m0, %[ldsw], 16384\n" \
  "v_mfma_f32_16x16x32_bf16 v[20:23], v[84:87], v[176:179], v[20:23]\nds_read_b64_tr_b16 v[116:117], v212 offset:40960\n" \
  "v_mfma_f32_16x16x32_bf16 v[52:55], v[84:87], v[180:183], v[52:55]\nds_read_b64_tr_b16 v[118:119], v213 offset:40960\nbuffer_load_dwordx4 v208, %[srdW], %[sW] offen offset:0 lds\n" \
  "v_mfma_f32_16x16x32_bf16 v[24:27], v[88:91], v[176:179], v[24:27]\nds_read_b64_tr_b16 v[120:121], v214 offset:40960\n" \
  "v_mfma_f32_16x16x32_bf16 v[56:59], v[88:91], v[180:183], v[56:59]\nds_read_b64_tr_b16 v[122:123], v215 offset:40960\n" \
  "v_mfma_f32_16x16x32_bf16 v[28:31], v[92:95], v[176:179], v[28:31]\nds_read_b64_tr_b16 v[124:125], v216 offset:40960\n" \
  "v_mfma_f32_16x16x32_bf16 v[60:63], v[92:95], v[180:183], v[60:63]\nds_read_b64_tr_b16 v[126:127], v217 offset:40960\n" \
  "buffer_load_dwordx4 v209, %[srdW], %[sW] offen offset:1024 lds\ns_waitcnt lgkmcnt(8)\nv_mfma_f32_16x16x32_bf16 v[32:35], v[96:99], v[176:179], v[32:35]\n" \
  "ds_read_b64_tr_b16 v[128:129], v218 offset:40960\nv_mfma_f32_16x16x32_bf16 v[64:67], v[96:99], v[180:183], v[64:67]\nds_read_b64_tr_b16 v[130:131], v219 offset:40960\n" \
  "s_add_u32 %[sW], %[sW], %[incW]\nv_mfma_f32_16x16x32_bf16 v[36:39], v[100:103], v[176:179], v[36:39]\nds_read_b64_tr_b16 v[132:133], v220 offset:40960\n" \
  "v_mfma_f32_16x16x32_bf16 v[68:71], v[100:103], v[180:183], v[68:71]\nds_read_b64_tr_b16 v[134:135], v221 offset:40960\n" \
  "buffer_load_dwordx4 v[160:163], %[vH], %[srdH], %[sH] offen offset:0 sc1\nv_mfma_f32_16x16x32_bf16 v[40:43], v[104:107], v[176:179], v[40:43]\n" \
  "ds_read_b64_tr_b16 v[136:137], v222 offset:40960\nv_mfma_f32_16x16x32_bf16 v[72:75], v[104:107], v[180:183], v[72:75]\nds_read_b64_tr_b16 v[138:139], v223 offset:40960\n" \
  "v_mfma_f32_16x16x32_bf16 v[44:47], v[108:111], v[176:179], v[44:47]\nds_read_b64_tr_b16 v[140:141], v224 offset:40960\n" \
  "v_mfma_f32_16x16x32_bf16 v[76:79], v[108:111], v[180:183], v[76:79]\nds_read_b64_tr_b16 v[142:143], v225 offset:40960\n" \
  "buffer_load_dwordx4 v[164:167], %[vH], %[srdH], %[sH] offen offset:1024 sc1\ns_add_u32 %[sH], %[sH], 8192\ns_waitcnt vmcnt(14)\ns_barrier\ns_waitcnt lgkmcnt(8)\n" \
  "v_mfma_f32_16x16x32_bf16 v[16:19], v[112:115], v[184:187], v[16:19]\nds_read_b64_tr_b16 v[80:81], v210 offset:49152\n" \
  "v_mfma_f32_16x16x32_bf16 v[48:51], v[112:115], v[188:191], v[48:51]\nds_read_b64_tr_b16 v[82:83], v211 offset:49152\ns_add_u32 m0, %[ldsw], 24576\n" \
  "v_mfma_f32_16x16x32_bf16 v[20:23], v[116:119], v[184:187], v[20:23]\nds_read_b64_tr_b16 v[84:85], v212 offset:49152\n" \
  "v_mfma_f32_16x16x32_bf16 v[52:55], v[116:119], v[188:191], v[52:55]\nds_read_b64_tr_b16 v[86:87], v213 offset:49152\nbuffer_load_dwordx4 v208, %[srdW], %[sW] offen offset:0 lds\n" \
  "v_mfma_f32_16x16x32_bf16 v[24:27], v[120:123], v[184:187], v[24:27]\nds_read_b64_tr_b16 v[88:89], v214 offset:49152\n" \
  "v_mfma_f32_16x16x32_bf16 v[56:59], v[120:123], v[188:191], v[56:59]\nds_read_b64_tr_b16 v[90:91], v215 offset:49152\n" \
  "v_mfma_f32_16x16x32_bf16 v[28:31], v[124:127], v[184:187], v[28:31]\nds_read_b64_tr_b16 v[92:93], v216 offset:49152\n" \
  "v_mfma_f32_16x16x32_bf16 v[60:63], v[124:127], v[188:191], v[60:63]\nds_read_b64_tr_b16 v[94:95], v217 offset:49152\n" \
  "buffer_load_dwordx4 v209, %[srdW], %[sW] offen offset:1024 lds\ns_waitcnt lgkmcnt(8)\nv_mfma_f32_16x16x32_bf16 v[32:35], v[128:131], v[184:187], v[32:35]\n" \
  "ds_read_b64_tr_b16 v[96:97], v218 offset:49152\nv_mfma_f32_16x16x32_bf16 v[64:67], v[128:131], v[188:191], v[64:67]\nds_read_b64_tr_b16 v[98:99], v219 offset:49152\n" \
  "s_add_u32 %[sW], %[sW], %[incW]\nv_mfma_f32_16x16x32_bf16 v[36:39], v[132:135], v[184:187], v[36:39]\nds_read_b64_tr_b16 v[100:101], v220 offset:49152\n" \
  "v_mfma_f32_16x16x32_bf16 v[68:71], v[132:135], v[188:191], v[68:71]\nds_read_b64_tr_b16 v[102:103], v221 offset:49152\n" \
  "buffer_load_dwordx4 v[168:171], %[vH], %[srdH], %[sH] offen offset:0 sc1\nv_mfma_f32_16x16x32_bf16 v[40:43], v[136:139], v[184:187], v[40:43]\n" \
  "ds_read_b64_tr_b16 v[104:105], v222 offset:49152\nv_mfma_f32_16x16x32_bf16 v[72:75], v[136:139], v[188:191], v[72:75]\nds_read_b64_tr_b16 v[106:107], v223 offset:49152\n" \
  "v_mfma_f32_16x16x32_bf16 v[44:47], v[140:143], v[184:187], v[44:47]\nds_read_b64_tr_b16 v[108:109], v224 offset:49152\n" \
  "v_mfma_f32_16x16x32_bf16 v[76:79], v[140:143], v[188:191], v[76:79]\nds_read_b64_tr_b16 v[110:111], v225 offset:49152\n" \
  "buffer_load_dwordx4 v[172:175], %[vH], %[srdH], %[sH] offen offset:1024 sc1\ns_add_u32 %[sH], %[sH], 8192\ns_waitcnt vmcnt(20)\ns_waitcnt lgkmcnt(8)\n" \
  "v_mfma_f32_16x16x32_bf16 v[16:19], v[80:83], v[192:195], v[16:19]\nds_read_b64_tr_b16 v[112:113], v210 offset:57344\n" \
  "v_mfma_f32_16x16x32_bf16 v[48:51], v[80:83], v[196:199], v[48:51]\nds_read_b64_tr_b16 v[114:115], v211 offset:57344\ns_add_u32 m0, %[ldsw], 32768\n" \
  "v_mfma_f32_16x16x32_bf16 v[20:23], v[84:87], v[192:195], v[20:23]\nds_read_b64_tr_b16 v[116:117], v212 offset:57344\n" \
  "v_mfma_f32_16x16x32_bf16 v[52:55], v[84:87], v[196:199], v[52:55]\nds_read_b64_tr_b16 v[118:119], v213 offset:57344\nbuffer_load_dwordx4 v208, %[srdW], %[sW] offen offset:0 lds\n" \
  "v_mfma_f32_16x16x32_bf16 v[24:27], v[88:91], v[192:195], v[24:27]\nds_read_b64_tr_b16 v[120:121], v214 offset:57344\n" \
  "v_mfma_f32_16x16x32_bf16 v[56:59], v[88:91], v[196:199], v[56:59]\nds_read_b64_tr_b16 v[122:123], v215 offset:57344\n" \
  "v_mfma_f32_16x16x32_bf16 v[28:31], v[92:95], v[192:195], v[28:31]\nds_read_b64_tr_b16 v[124:125], v216 offset:57344\n" \
  "v_mfma_f32_16x16x32_bf16 v[60:63], v[92:95], v[196:199], v[60:63]\nds_read_b64_tr_b16 v[126:127], v217 offset:57344\n" \
  "buffer_load_dwordx4 v209, %[srdW], %[sW] offen offset:1024 lds\ns_waitcnt lgkmcnt(8)\nv_mfma_f32_16x16x32_bf16 v[32:35], v[96:99], v[192:195], v[32:35]\n" \
  "ds_read_b64_tr_b16 v[128:129], v218 offset:57344\nv_mfma_f32_16x16x32_bf16 v[64:67], v[96:99], v[196:199], v[64:67]\nds_read_b64_tr_b16 v[130:131], v219 offset:57344\n" \
  "s_add_u32 %[sW], %[sW], %[incW]\nv_mfma_f32_16x16x32_bf16 v[36:39], v[100:103], v[192:195], v[36:39]\nds_read_b64_tr_b16 v[132:133], v220 offset:57344\n" \
  "v_mfma_f32_16x16x32_bf16 v[68:71], v[100:103], v[196:199], v[68:71]\nds_read_b64_tr_b16 v[134:135], v221 offset:57344\n" \
  "buffer_load_dwordx4 v[176:179], %[vH], %[srdH], %[sH] offen offset:0 sc1\nv_mfma_f32_16x16x32_bf16 v[40:43], v[104:107], v[192:195], v[40:43]\n" \
  "ds_read_b64_tr_b16 v[136:137], v222 offset:57344\nv_mfma_f32_16x16x32_bf16 v[72:75], v[104:107], v[196:199], v[72:75]\nds_read_b64_tr_b16 v[138:139], v223 offset:57344\n" \
  "v_mfma_f32_16x16x32_bf16 v[44:47], v[108:111], v[192:195], v[44:47]\nds_read_b64_tr_b16 v[140:141], v224 offset:57344\n" \
  "v_mfma_f32_16x16x32_bf16 v[76:79], v[108:111], v[196:199], v[76:79]\nds_read_b64_tr_b16 v[142:143], v225 offset:57344\n" \
  "buffer_load_dwordx4 v[180:183], %[vH], %[srdH], %[sH] offen offset:1024 sc1\ns_add_u32 %[sH], %[sH], 8192\ns_waitcnt vmcnt(14)\ns_barrier\ns_waitcnt lgkmcnt(8)\n" \
  "v_mfma_f32_16x16x32_bf16 v[16:19], v[112:115], v[200:203], v[16:19]\nds_read_b64_tr_b16 v[80:81], v210 offset:0\n" \
  "v_mfma_f32_16x16x32_bf16 v[48:51], v[112:115], v[204:207], v[48:51]\nds_read_b64_tr_b16 v[82:83], v211 offset:0\ns_add_u32 m0, %[ldsw], 40960\n" \
  "v_mfma_f32_16x16x32_bf16 v[20:23], v[116:119], v[200:203], v[20:23]\nds_read_b64_tr_b16 v[84:85], v212 offset:0\n" \
  "v_mfma_f32_16x16x32_bf16 v[52:55], v[116:119], v[204:207], v[52:55]\nds_read_b64_tr_b16 v[86:87], v213 offset:0\nbuffer_load_dwordx4 v208, %[srdW], %[sW] offen offset:0 lds\n" \
  "v_mfma_f32_16x16x32_bf16 v[24:27], v[120:123], v[200:203], v[24:27]\nds_read_b64_tr_b16 v[88:89], v214 offset:0\n" \
  "v_mfma_f32_16x16x32_bf16 v[56:59], v[120:123], v[204:207], v[56:59]\nds_read_b64_tr_b16 v[90:91], v215 offset:0\n" \
  "v_mfma_f32_16x16x32_bf16 v[28:31], v[124:127], v[200:203], v[28:31]\nds_read_b64_tr_b16 v[92:93], v216 offset:0\n" \
  "v_mfma_f32_16x16x32_bf16 v[60:63], v[124:127], v[204:207], v[60:63]\nds_read_b64_tr_b16 v[94:95], v217 offset:0\nbuffer_load_dwordx4 v209, %[srdW], %[sW] offen offset:1024 lds\n" \
  "s_waitcnt lgkmcnt(8)\nv_mfma_f32_16x16x32_bf16 v[32:35], v[128:131], v[200:203], v[32:35]\nds_read_b64_tr_b16 v[96:97], v218 offset:0\n" \
  "v_mfma_f32_16x16x32_bf16 v[64:67], v[128:131], v[204:207], v[64:67]\nds_read_b64_tr_b16 v[98:99], v219 offset:0\ns_add_u32 %[sW], %[sW], %[incW]\n" \
  "v_mfma_f32_16x16x32_bf16 v[36:39], v[132:135], v[200:203], v[36:39]\nds_read_b64_tr_b16 v[100:101], v220 offset:0\n" \
  "v_mfma_f32_16x16x32_bf16 v[68:71], v[132:135], v[204:207], v[68:71]\nds_read_b64_tr_b16 v[102:103], v221 offset:0\n" \
  "buffer_load_dwordx4 v[184:187], %[vH], %[srdH], %[sH] offen offset:0 sc1\nv_mfma_f32_16x16x32_bf16 v[40:43], v[136:139], v[200:203], v[40:43]\n" \
  "ds_read_b64_tr_b16 v[104:105], v222 offset:0\nv_mfma_f32_16x16x32_bf16 v[72:75], v[136:139], v[204:207], v[72:75]\nds_read_b64_tr_b16 v[106:107], v223 offset:0\n" \
  "v_mfma_f32_16x16x32_bf16 v[44:47], v[140:143], v[200:203], v[44:47]\nds_read_b64_tr_b16 v[108:109], v224 offset:0\n" \
  "v_mfma_f32_16x16x32_bf16 v[76:79], v[140:143], v[204:207], v[76:79]\nds_read_b64_tr_b16 v[110:111], v225 offset:0\n" \
  "buffer_load_dwordx4 v[188:191], %[vH], %[srdH], %[sH] offen offset:1024 sc1\ns_add_u32 %[sH], %[sH], 8192\ns_sub_u32 %[cnt], %[cnt], 1\ns_cmp_lg_u32 %[cnt], 0\n" \
  "s_cbranch_scc1 Lk%=\ns_waitcnt vmcnt(20)\ns_waitcnt lgkmcnt(8)\nv_mfma_f32_16x16x32_bf16 v[16:19], v[80:83], v[144:147], v[16:19]\nds_read_b64_tr_b16 v[112:113], v210 offset:8192\n" \
  "v_mfma_f32_16x16x32_bf16 v[48:51], v[80:83], v[148:151], v[48:51]\nds_read_b64_tr_b16 v[114:115], v211 offset:8192\ns_add_u32 m0, %[ldsw], 49152\n" \
  "v_mfma_f32_16x16x32_bf16 v[20:23], v[84:87], v[144:147], v[20:23]\nds_read_b64_tr_b16 v[116:117], v212 offset:8192\n" \
  "v_mfma_f32_16x16x32_bf16 v[52:55], v[84:87], v[148:151], v[52:55]\nds_read_b64_tr_b16 v[118:119], v213 offset:8192\nbuffer_load_dwordx4 v208, %[srdW], %[sW] offen offset:0 lds\n" \
  "v_mfma_f32_16x16x32_bf16 v[24:27], v[88:91], v[144:147], v[24:27]\nds_read_b64_tr_b16 v[120:121], v214 offset:8192\n" \
  "v_mfma_f32_16x16x32_bf16 v[56:59], v[88:91], v[148:151], v[56:59]\nds_read_b64_tr_b16 v[122:123], v215 offset:8192\n" \
  "v_mfma_f32_16x16x32_bf16 v[28:31], v[92:95], v[144:147], v[28:31]\nds_read_b64_tr_b16 v[124:125], v216 offset:8192\n" \
  "v_mfma_f32_16x16x32_bf16 v[60:63], v[92:95], v[148:151], v[60:63]\nds_read_b64_tr_b16 v[126:127], v217 offset:8192\nbuffer_load_dwordx4 v209, %[srdW], %[sW] offen offset:1024 lds\n" \
  "s_waitcnt lgkmcnt(8)\nv_mfma_f32_16x16x32_bf16 v[32:35], v[96:99], v[144:147], v[32:35]\nds_read_b64_tr_b16 v[128:129], v218 offset:8192\n" \
  "v_mfma_f32_16x16x32_bf16 v[64:67], v[96:99], v[148:151], v[64:67]\nds_read_b64_tr_b16 v[130:131], v219 offset:8192\ns_add_u32 %[sW], %[sW], %[incW]\n" \
  "v_mfma_f32_16x16x32_bf16 v[36:39], v[100:103], v[144:147], v[36:39]\nds_read_b64_tr_b16 v[132:133], v220 offset:8192\n" \
  "v_mfma_f32_16x16x32_bf16 v[68:71], v[100:103], v[148:151], v[68:71]\nds_read_b64_tr_b16 v[134:135], v221 offset:8192\n" \
  "buffer_load_dwordx4 v[192:195], %[vH], %[srdH], %[sH] offen offset:0 sc1\nv_mfma_f32_16x16x32_bf16 v[40:43], v[104:107], v[144:147], v[40:43]\n" \
  "ds_read_b64_tr_b16 v[136:137], v222 offset:8192\nv_mfma_f32_16x16x32_bf16 v[72:75], v[104:107], v[148:151], v[72:75]\nds_read_b64_tr_b16 v[138:139], v223 offset:8192\n" \
  "v_mfma_f32_16x16x32_bf16 v[44:47], v[108:111], v[144:147], v[44:47]\nds_read_b64_tr_b16 v[140:141], v224 offset:8192\n" \
  "v_mfma_f32_16x16x32_bf16 v[76:79], v[108:111], v[148:151], v[76:79]\nds_read_b64_tr_b16 v[142:143], v225 offset:8192\n" \
  "buffer_load_dwordx4 v[196:199], %[vH], %[srdH], %[sH] offen offset:1024 sc1\ns_add_u32 %[sH], %[sH], 8192\ns_waitcnt vmcnt(14)\ns_barrier\ns_waitcnt lgkmcnt(8)\n" \
  "v_mfma_f32_16x16x32_bf16 v[16:19], v[112:115], v[152:155], v[16:19]\nds_read_b64_tr_b16 v[80:81], v210 offset:16384\n" \
  "v_mfma_f32_16x16x32_bf16 v[48:51], v[112:115], v[156:159], v[48:51]\nds_read_b64_tr_b16 v[82:83], v211 offset:16384\ns_add_u32 m0, %[ldsw], 57344\n" \
  "v_mfma_f32_16x16x32_bf16 v[20:23], v[116:119], v[152:155], v[20:23]\nds_read_b64_tr_b16 v[84:85], v212 offset:16384\n" \
  "v_mfma_f32_16x16x32_bf16 v[52:55], v[116:119], v[156:159], v[52:55]\nds_read_b64_tr_b16 v[86:87], v213 offset:16384\nbuffer_load_dwordx4 v208, %[srdW], %[sW] offen offset:0 lds\n" \
  "v_mfma_f32_16x16x32_bf16 v[24:27], v[120:123], v[152:155], v[24:27]\nds_read_b64_tr_b16 v[88:89], v214 offset:16384\n" \
  "v_mfma_f32_16x16x32_bf16 v[56:59], v[120:123], v[156:159], v[56:59]\nds_read_b64_tr_b16 v[90:91], v215 offset:16384\n" \
  "v_mfma_f32_16x16x32_bf16 v[28:31], v[124:127], v[152:155], v[28:31]\nds_read_b64_tr_b16 v[92:93], v216 offset:16384\n" \
  "v_mfma_f32_16x16x32_bf16 v[60:63], v[124:127], v[156:159], v[60:63]\nds_read_b64_tr_b16 v[94:95], v217 offset:16384\n" \
  "buffer_load_dwordx4 v209, %[srdW], %[sW] offen offset:1024 lds\ns_waitcnt lgkmcnt(8)\nv_mfma_f32_16x16x32_bf16 v[32:35], v[128:131], v[152:155], v[32:35]\n" \
  "ds_read_b64_tr_b16 v[96:97], v218 offset:16384\nv_mfma_f32_16x16x32_bf16 v[64:67], v[128:131], v[156:159], v[64:67]\nds_read_b64_tr_b16 v[98:99], v219 offset:16384\n" \
  "s_add_u32 %[sW], %[sW], %[incW]\nv_mfma_f32_16x16x32_bf16 v[36:39], v[132:135], v[152:155], v[36:39]\nds_read_b64_tr_b16 v[100:101], v220 offset:16384\n" \
  "v_mfma_f32_16x16x32_bf16 v[68:71], v[132:135], v[156:159], v[68:71]\nds_read_b64_tr_b16 v[102:103], v221 offset:16384\n" \
  "buffer_load_dwordx4 v[200:203], %[vH], %[srdH], %[sH] offen offset:0 sc1\nv_mfma_f32_16x16x32_bf16 v[40:43], v[136:139], v[152:155], v[40:43]\n" \
  "ds_read_b64_tr_b16 v[104:105], v222 offset:16384\nv_mfma_f32_16x16x32_bf16 v[72:75], v[136:139], v[156:159], v[72:75]\nds_read_b64_tr_b16 v[106:107], v223 offset:16384\n" \
  "v_mfma_f32_16x16x32_bf16 v[44:47], v[140:143], v[152:155], v[44:47]\nds_read_b64_tr_b16 v[108:109], v224 offset:16384\n" \
  "v_mfma_f32_16x16x32_bf16 v[76:79], v[140:143], v[156:159], v[76:79]\nds_read_b64_tr_b16 v[110:111], v225 offset:16384\n" \
  "buffer_load_dwordx4 v[204:207], %[vH], %[srdH], %[sH] offen offset:1024 sc1\ns_add_u32 %[sH], %[sH], 8192\ns_waitcnt vmcnt(20)\ns_waitcnt lgkmcnt(8)\n" \
  "v_mfma_f32_16x16x32_bf16 v[16:19], v[80:83], v[160:163], v[16:19]\nds_read_b64_tr_b16 v[112:113], v210 offset:24576\n" \
  "v_mfma_f32_16x16x32_bf16 v[48:51], v[80:83], v[164:167], v[48:51]\nds_read_b64_tr_b16 v[114:115], v211 offset:24576\n" \
  "v_mfma_f32_16x16x32_bf16 v[20:23], v[84:87], v[160:163], v[20:23]\nds_read_b64_tr_b16 v[116:117], v212 offset:24576\n" \
  "v_mfma_f32_16x16x32_bf16 v[52:55], v[84:87], v[164:167], v[52:55]\nds_read_b64_tr_b16 v[118:119], v213 offset:24576\n" \
  "v_mfma_f32_16x16x32_bf16 v[24:27], v[88:91], v[160:163], v[24:27]\nds_read_b64_tr_b16 v[120:121], v214 offset:24576\n" \
  "v_mfma_f32_16x16x32_bf16 v[56:59], v[88:91], v[164:167], v[56:59]\nds_read_b64_tr_b16 v[122:123], v215 offset:24576\n" \
  "v_mfma_f32_16x16x32_bf16 v[28:31], v[92:95], v[160:163], v[28:31]\nds_read_b64_tr_b16 v[124:125], v216 offset:24576\n" \
  "v_mfma_f32_16x16x32_bf16 v[60:63], v[92:95], v[164:167], v[60:63]\nds_read_b64_tr_b16 v[126:127], v217 offset:24576\ns_waitcnt lgkmcnt(8)\n" \
  "v_mfma_f32_16x16x32_bf16 v[32:35], v[96:99], v[160:163], v[32:35]\nds_read_b64_tr_b16 v[128:129], v218 offset:24576\n" \
  "v_mfma_f32_16x16x32_bf16 v[64:67], v[96:99], v[164:167], v[64:67]\nds_read_b64_tr_b16 v[130:131], v219 offset:24576\n" \
  "v_mfma_f32_16x16x32_bf16 v[36:39], v[100:103], v[160:163], v[36:39]\nds_read_b64_tr_b16 v[132:133], v220 offset:24576\n" \
  "v_mfma_f32_16x16x32_bf16 v[68:71], v[100:103], v[164:167], v[68:71]\nds_read_b64_tr_b16 v[134:135], v221 offset:24576\n" \
  "v_mfma_f32_16x16x32_bf16 v[40:43], v[104:107], v[160:163], v[40:43]\nds_read_b64_tr_b16 v[136:137], v222 offset:24576\n" \
  "v_mfma_f32_16x16x32_bf16 v[72:75], v[104:107], v[164:167], v[72:75]\nds_read_b64_tr_b16 v[138:139], v223 offset:24576\n" \
  "v_mfma_f32_16x16x32_bf16 v[44:47], v[108:111], v[160:163], v[44:47]\nds_read_b64_tr_b16 v[140:141], v224 offset:24576\n" \
  "v_mfma_f32_16x16x32_bf16 v[76:79], v[108:111], v[164:167], v[76:79]\nds_read_b64_tr_b16 v[142:143], v225 offset:24576\ns_waitcnt vmcnt(10)\ns_barrier\ns_waitcnt lgkmcnt(8)\n" \
  "v_mfma_f32_16x16x32_bf16 v[16:19], v[112:115], v[168:171], v[16:19]\nds_read_b64_tr_b16 v[80:81], v210 offset:32768\n" \
  "v_mfma_f32_16x16x32_bf16 v[48:51], v[112:115], v[172:175], v[48:51]\nds_read_b64_tr_b16 v[82:83], v211 offset:32768\n" \
  "v_mfma_f32_16x16x32_bf16 v[20:23], v[116:119], v[168:171], v[20:23]\nds_read_b64_tr_b16 v[84:85], v212 offset:32768\n" \
  "v_mfma_f32_16x16x32_bf16 v[52:55], v[116:119], v[172:175], v[52:55]\nds_read_b64_tr_b16 v[86:87], v213 offset:32768\n" \
  "v_mfma_f32_16x16x32_bf16 v[24:27], v[120:123], v[168:171], v[24:27]\nds_read_b64_tr_b16 v[88:89], v214 offset:32768\n" \
  "v_mfma_f32_16x16x32_bf16 v[56:59], v[120:123], v[172:175], v[56:59]\nds_read_b64_tr_b16 v[90:91], v215 offset:32768\n" \
  "v_mfma_f32_16x16x32_bf16 v[28:31], v[124:127], v[168:171], v[28:31]\nds_read_b64_tr_b16 v[92:93], v216 offset:32768\n" \
  "v_mfma_f32_16x16x32_bf16 v[60:63], v[124:127], v[172:175], v[60:63]\nds_read_b64_tr_b16 v[94:95], v217 offset:32768\ns_waitcnt lgkmcnt(8)\n" \
  "v_mfma_f32_16x16x32_bf16 v[32:35], v[128:131], v[168:171], v[32:35]\nds_read_b64_tr_b16 v[96:97], v218 offset:32768\n" \
  "v_mfma_f32_16x16x32_bf16 v[64:67], v[128:131], v[172:175], v[64:67]\nds_read_b64_tr_b16 v[98:99], v219 offset:32768\n" \
  "v_mfma_f32_16x16x32_bf16 v[36:39], v[132:135], v[168:171], v[36:39]\nds_read_b64_tr_b16 v[100:101], v220 offset:32768\n" \
  "v_mfma_f32_16x16x32_bf16 v[68:71], v[132:135], v[172:175], v[68:71]\nds_read_b64_tr_b16 v[102:103], v221 offset:32768\n" \
  "v_mfma_f32_16x16x32_bf16 v[40:43], v[136:139], v[168:171], v[40:43]\nds_read_b64_tr_b16 v[104:105], v222 offset:32768\n" \
  "v_mfma_f32_16x16x32_bf16 v[72:75], v[136:139], v[172:175], v[72:75]\nds_read_b64_tr_b16 v[106:107], v223 offset:32768\n" \
  "v_mfma_f32_16x16x32_bf16 v[44:47], v[140:143], v[168:171], v[44:47]\nds_read_b64_tr_b16 v[108:109], v224 offset:32768\n" \
  "v_mfma_f32_16x16x32_bf16 v[76:79], v[140:143], v[172:175], v[76:79]\nds_read_b64_tr_b16 v[110:111], v225 offset:32768\ns_waitcnt vmcnt(12)\ns_waitcnt lgkmcnt(8)\n" \
  "v_mfma_f32_16x16x32_bf16 v[16:19], v[80:83], v[176:179], v[16:19]\nds_read_b64_tr_b16 v[112:113], v210 offset:40960\n" \
  "v_mfma_f32_16x16x32_bf16 v[48:51], v[80:83], v[180:183], v[48:51]\nds_read_b64_tr_b16 v[114:115], v211 offset:40960\n" \
  "v_mfma_f32_16x16x32_bf16 v[20:23], v[84:87], v[176:179], v[20:23]\nds_read_b64_tr_b16 v[116:117], v212 offset:40960\n" \
  "v_mfma_f32_16x16x32_bf16 v[52:55], v[84:87], v[180:183], v[52:55]\nds_read_b64_tr_b16 v[118:119], v213 offset:40960\n" \
  "v_mfma_f32_16x16x32_bf16 v[24:27], v[88:91], v[176:179], v[24:27]\nds_read_b64_tr_b16 v[120:121], v214 offset:40960\n" \
  "v_mfma_f32_16x16x32_bf16 v[56:59], v[88:91], v[180:183], v[56:59]\nds_read_b64_tr_b16 v[122:123], v215 offset:40960\n" \
  "v_mfma_f32_16x16x32_bf16 v[28:31], v[92:95], v[176:179], v[28:31]\nds_read_b64_tr_b16 v[124:125], v216 offset:40960\n" \
  "v_mfma_f32_16x16x32_bf16 v[60:63], v[92:95], v[180:183], v[60:63]\nds_read_b64_tr_b16 v[126:127], v217 offset:40960\ns_waitcnt lgkmcnt(8)\n" \
  "v_mfma_f32_16x16x32_bf16 v[32:35], v[96:99], v[176:179], v[32:35]\nds_read_b64_tr_b16 v[128:129], v218 offset:40960\n" \
  "v_mfma_f32_16x16x32_bf16 v[64:67], v[96:99], v[180:183], v[64:67]\nds_read_b64_tr_b16 v[130:131], v219 offset:40960\n" \
  "v_mfma_f32_16x16x32_bf16 v[36:39], v[100:103], v[176:179], v[36:39]\nds_read_b64_tr_b16 v[132:133], v220 offset:40960\n" \
  "v_mfma_f32_16x16x32_bf16 v[68:71], v[100:103], v[180:183], v[68:71]\nds_read_b64_tr_b16 v[134:135], v221 offset:40960\n" \
  "v_mfma_f32_16x16x32_bf16 v[40:43], v[104:107], v[176:179], v[40:43]\nds_read_b64_tr_b16 v[136:137], v222 offset:40960\n" \
  "v_mfma_f32_16x16x32_bf16 v[72:75], v[104:107], v[180:183], v[72:75]\nds_read_b64_tr_b16 v[138:139], v223 offset:40960\n" \
  "v_mfma_f32_16x16x32_bf16 v[44:47], v[108:111], v[176:179], v[44:47]\nds_read_b64_tr_b16 v[140:141], v224 offset:40960\n" \
  "v_mfma_f32_16x16x32_bf16 v[76:79], v[108:111], v[180:183], v[76:79]\nds_read_b64_tr_b16 v[142:143], v225 offset:40960\ns_waitcnt vmcnt(2)\ns_barrier\ns_waitcnt lgkmcnt(8)\n" \
  "v_mfma_f32_16x16x32_bf16 v[16:19], v[112:115], v[184:187], v[16:19]\nds_read_b64_tr_b16 v[80:81], v210 offset:49152\n" \
  "v_mfma_f32_16x16x32_bf16 v[48:51], v[112:115], v[188:191], v[48:51]\nds_read_b64_tr_b16 v[82:83], v211 offset:49152\n" \
  "v_mfma_f32_16x16x32_bf16 v[20:23], v[116:119], v[184:187], v[20:23]\nds_read_b64_tr_b16 v[84:85], v212 offset:49152\n" \
  "v_mfma_f32_16x16x32_bf16 v[52:55], v[116:119], v[188:191], v[52:55]\nds_read_b64_tr_b16 v[86:87], v213 offset:49152\n" \
  "v_mfma_f32_16x16x32_bf16 v[24:27], v[120:123], v[184:187], v[24:27]\nds_read_b64_tr_b16 v[88:89], v214 offset:49152\n" \
  "v_mfma_f32_16x16x32_bf16 v[56:59], v[120:123], v[188:191], v[56:59]\nds_read_b64_tr_b16 v[90:91], v215 offset:49152\n" \
  "v_mfma_f32_16x16x32_bf16 v[28:31], v[124:127], v[184:187], v[28:31]\nds_read_b64_tr_b16 v[92:93], v216 offset:49152\n" \
  "v_mfma_f32_16x16x32_bf16 v[60:63], v[124:127], v[188:191], v[60:63]\nds_read_b64_tr_b16 v[94:95], v217 offset:49152\ns_waitcnt lgkmcnt(8)\n" \
  "v_mfma_f32_16x16x32_bf16 v[32:35], v[128:131], v[184:187], v[32:35]\nds_read_b64_tr_b16 v[96:97], v218 offset:49152\n" \
  "v_mfma_f32_16x16x32_bf16 v[64:67], v[128:131], v[188:191], v[64:67]\nds_read_b64_tr_b16 v[98:99], v219 offset:49152\n" \
  "v_mfma_f32_16x16x32_bf16 v[36:39], v[132:135], v[184:187], v[36:39]\nds_read_b64_tr_b16 v[100:101], v220 offset:49152\n" \
  "v_mfma_f32_16x16x32_bf16 v[68:71], v[132:135], v[188:191], v[68:71]\nds_read_b64_tr_b16 v[102:103], v221 offset:49152\n" \
  "v_mfma_f32_16x16x32_bf16 v[40:43], v[136:139], v[184:187], v[40:43]\nds_read_b64_tr_b16 v[104:105], v222 offset:49152\n" \
  "v_mfma_f32_16x16x32_bf16 v[72:75], v[136:139], v[188:191], v[72:75]\nds_read_b64_tr_b16 v[106:107], v223 offset:49152\n" \
  "v_mfma_f32_16x16x32_bf16 v[44:47], v[140:143], v[184:187], v[44:47]\nds_read_b64_tr_b16 v[108:109], v224 offset:49152\n" \
  "v_mfma_f32_16x16x32_bf16 v[76:79], v[140:143], v[188:191], v[76:79]\nds_read_b64_tr_b16 v[110:111], v225 offset:49152\ns_waitcnt vmcnt(4)\ns_waitcnt lgkmcnt(8)\n" \
  "v_mfma_f32_16x16x32_bf16 v[16:19], v[80:83], v[192:195], v[16:19]\nds_read_b64_tr_b16 v[112:113], v210 offset:57344\n" \
  "v_mfma_f32_16x16x32_bf16 v[48:51], v[80:83], v[196:199], v[48:51]\nds_read_b64_tr_b16 v[114:115], v211 offset:57344\n" \
  "v_mfma_f32_16x16x32_bf16 v[20:23], v[84:87], v[192:195], v[20:23]\nds_read_b64_tr_b16 v[116:117], v212 offset:57344\n" \
  "v_mfma_f32_16x16x32_bf16 v[52:55], v[84:87], v[196:199], v[52:55]\nds_read_b64_tr_b16 v[118:119], v213 offset:57344\n" \
  "v_mfma_f32_16x16x32_bf16 v[24:27], v[88:91], v[192:195], v[24:27]\nds_read_b64_tr_b16 v[120:121], v214 offset:57344\n" \
  "v_mfma_f32_16x16x32_bf16 v[56:59], v[88:91], v[196:199], v[56:59]\nds_read_b64_tr_b16 v[122:123], v215 offset:57344\n" \
  "v_mfma_f32_16x16x32_bf16 v[28:31], v[92:95], v[192:195], v[28:31]\nds_read_b64_tr_b16 v[124:125], v216 offset:57344\n" \
  "v_mfma_f32_16x16x32_bf16 v[60:63], v[92:95], v[196:199], v[60:63]\nds_read_b64_tr_b16 v[126:127], v217 offset:57344\ns_waitcnt lgkmcnt(8)\n" \
  "v_mfma_f32_16x16x32_bf16 v[32:35], v[96:99], v[192:195], v[32:35]\nds_read_b64_tr_b16 v[128:129], v218 offset:57344\n" \
  "v_mfma_f32_16x16x32_bf16 v[64:67], v[96:99], v[196:199], v[64:67]\nds_read_b64_tr_b16 v[130:131], v219 offset:57344\n" \
  "v_mfma_f32_16x16x32_bf16 v[36:39], v[100:103], v[192:195], v[36:39]\nds_read_b64_tr_b16 v[132:133], v220 offset:57344\n" \
  "v_mfma_f32_16x16x32_bf16 v[68:71], v[100:103], v[196:199], v[68:71]\nds_read_b64_tr_b16 v[134:135], v221 offset:57344\n" \
  "v_mfma_f32_16x16x32_bf16 v[40:43], v[104:107], v[192:195], v[40:43]\nds_read_b64_tr_b16 v[136:137], v222 offset:57344\n" \
  "v_mfma_f32_16x16x32_bf16 v[72:75], v[104:107], v[196:199], v[72:75]\nds_read_b64_tr_b16 v[138:139], v223 offset:57344\n" \
  "v_mfma_f32_16x16x32_bf16 v[44:47], v[108:111], v[192:195], v[44:47]\nds_read_b64_tr_b16 v[140:141], v224 offset:57344\n" \
  "v_mfma_f32_16x16x32_bf16 v[76:79], v[108:111], v[196:199], v[76:79]\nds_read_b64_tr_b16 v[142:143], v225 offset:57344\ns_waitcnt vmcnt(0)\ns_waitcnt lgkmcnt(8)\n" \
  "v_mfma_f32_16x16x32_bf16 v[16:19], v[112:115], v[200:203], v[16:19]\nv_mfma_f32_16x16x32_bf16 v[48:51], v[112:115], v[204:207], v[48:51]\n" \
  "v_mfma_f32_16x16x32_bf16 v[20:23], v[116:119], v[200:203], v[20:23]\nv_mfma_f32_16x16x32_bf16 v[52:55], v[116:119], v[204:207], v[52:55]\n" \
  "v_mfma_f32_16x16x32_bf16 v[24:27], v[120:123], v[200:203], v[24:27]\nv_mfma_f32_16x16x32_bf16 v[56:59], v[120:123], v[204:207], v[56:59]\n" \
  "v_mfma_f32_16x16x32_bf16 v[28:31], v[124:127], v[200:203], v[28:31]\nv_mfma_f32_16x16x32_bf16 v[60:63], v[124:127], v[204:207], v[60:63]\ns_waitcnt lgkmcnt(0)\n" \
  "v_mfma_f32_16x16x32_bf16 v[32:35], v[128:131], v[200:203], v[32:35]\nv_mfma_f32_16x16x32_bf16 v[64:67], v[128:131], v[204:207], v[64:67]\n" \
  "v_mfma_f32_16x16x32_bf16 v[36:39], v[132:135], v[200:203], v[36:39]\nv_mfma_f32_16x16x32_bf16 v[68:71], v[132:135], v[204:207], v[68:71]\n" \
  "v_mfma_f32_16x16x32_bf16 v[40:43], v[136:139], v[200:203], v[40:43]\nv_mfma_f32_16x16x32_bf16 v[72:75], v[136:139], v[204:207], v[72:75]\n" \
  "v_mfma_f32_16x16x32_bf16 v[44:47], v[140:143], v[200:203], v[44:47]\nv_mfma_f32_16x16x32_bf16 v[76:79], v[140:143], v[204:207], v[76:79]\ns_nop 7\ns_nop 1\n"
#define W2_FIRST 3
#define GEMM1_CLOB \
  "v80","v81","v82","v83","v84","v85","v86","v87","v88","v89","v90","v91","v92","v93","v94","v95","v96","v97","v98","v99","v100","v101","v102","v103", \
  "v104","v105","v106","v107","v108","v109","v110","v111"
#define GEMM2_CLOB \
  "v80","v81","v82","v83","v84","v85","v86","v87","v88","v89","v90","v91","v92","v93","v94","v95","v96","v97","v98","v99","v100","v101","v102","v103", \
  "v104","v105","v106","v107","v108","v109","v110","v111","v112","v113","v114","v115","v116","v117","v118","v119","v120","v121","v122","v123","v124","v125","v126","v127", \
  "v128","v129","v130","v131","v132","v133","v134","v135","v136","v137","v138","v139","v140","v141","v142","v143"


DI unsigned pk(float lo, float hi) { f32x2 v = {lo, hi}; return __builtin_bit_cast(unsigned, __builtin_convertvector(v, bf16x2)); }
DI float rbf(float v) { return __uint_as_float(pk(v, v) << 16); }
DI float lo16(unsigned u) { return __uint_as_float(u << 16); }
DI float hi16(unsigned u) { return __uint_as_float(u & 0xffff0000u); }
DI f32x16 mfma32(u32x4 a, u32x4 b, f32x16 c) {
    return __builtin_amdgcn_mfma_f32_32x32x16_bf16(__builtin_bit_cast(bf16x8, a), __builtin_bit_cast(bf16x8, b), c, 0, 0, 0);
}
DI u32x2 trread(unsigned addr) { return __builtin_bit_cast(u32x2, __builtin_amdgcn_ds_read_tr16_b64_v4i16((LDS s16x4*)(size_t)addr)); }
DI u32x4 cat(u32x2 a, u32x2 b) { return u32x4{a[0], a[1], b[0], b[1]}; }
DI u32x4 pack8(const f32x16& x, int s) {
    return u32x4{pk(x[8 * s], x[8 * s + 1]), pk(x[8 * s + 2], x[8 * s + 3]), pk(x[8 * s + 4], x[8 * s + 5]), pk(x[8 * s + 6], x[8 * s + 7])};
}
DI void bar() { asm volatile("s_barrier" ::: "memory"); }
DI void lds_bar() { asm volatile("s_waitcnt lgkmcnt(0)\n s_barrier" ::: "memory"); }
DI void glds(const void* src, unsigned dst) { asm volatile("s_mov_b32 m0, %1\n s_nop 0\n global_load_lds_dwordx4 %0, off" ::"v"(src), "s"(dst) : "memory", "m0"); }
DI void touch(unsigned& sink, __amdgpu_buffer_rsrc_t r, unsigned off) { asm volatile("s_nop 4\n buffer_load_dword %0, %1, %2, 0 offen" : "+v"(sink) : "v"(off), "s"(r) : "memory"); }
DI __amdgpu_buffer_rsrc_t srd(const void* p, unsigned bytes) { return __builtin_amdgcn_make_buffer_rsrc((void*)p, 0, bytes, SRD3); }

template <bool PERM> DI u32x2 gemm_wsrc(int wave, int lane, unsigned row_bytes) {
    u32x2 r;
#pragma unroll
    for (int i = 0; i < 2; ++i) {
        const int row = 8 * wave + 4 * i + (lane >> 4), swz = PERM ? (row & 12) | (row & 1) : ((row & 3) << 2) | ((row >> 3) & 1);
        r[i] = (PERM ? (row & 3) + 32 * (row >> 2) : row) * row_bytes + (((lane & 15) ^ swz) << 4) - i * 1024;
    }
    return r;
}
template <bool PERM> DI unsigned gemm_rdaddr(unsigned lds, int lane, int e, int eflip, int half) {
    const int g = lane >> 4, q = (lane >> 2) & 3, p = lane & 3;
    return lds + 256 * (PERM ? 16 * half + 4 * q + g : 8 * g + 4 * half + q) + 64 * ((e >> 1) ^ eflip ^ q) + 16 * (p ^ (g & 1)) + 8 * ((e ^ p) & 1);
}
DI void st_dev(u32x4 v, __amdgpu_buffer_rsrc_t r, unsigned off) { __builtin_amdgcn_raw_buffer_store_b128(v, r, off, 0, 16); }
DI u32x4 ld_dev(__amdgpu_buffer_rsrc_t r, unsigned off) { return __builtin_amdgcn_raw_buffer_load_b128(r, off, 0, 16); }
DI unsigned gelu2(float u0, float u1) {
    const f32x2 u = {u0, u1}, z = u * __builtin_elementwise_fma(u * u, f32x2{-0.1029432397f, -0.1029432397f}, f32x2{-2.302208198f, -2.302208198f});
    const f32x2 d = f32x2{__builtin_amdgcn_exp2f(z[0]), __builtin_amdgcn_exp2f(z[1])} + 1.f;
    const f32x2 r = u * f32x2{__builtin_amdgcn_rcpf(d[0]), __builtin_amdgcn_rcpf(d[1])};
    return pk(r[0], r[1]);
}

typedef unsigned long long u64;
constexpr int SPIN_MAX = 1 << 22;
DI void set_flag(u64* f, u64 tok) { __hip_atomic_store(f, tok, __ATOMIC_RELAXED, __HIP_MEMORY_SCOPE_AGENT); }
DI u64 peek_flag(u64* f) { return __hip_atomic_load(f, __ATOMIC_RELAXED, __HIP_MEMORY_SCOPE_AGENT); }
DI void wait_flag(u64* f, u64 tok) {
#pragma unroll 1
    for (int spin = 0; spin < SPIN_MAX; ++spin) {
        if (__all(peek_flag(f) == tok)) break;
        __builtin_amdgcn_s_sleep(1);
    }
    __builtin_amdgcn_fence(__ATOMIC_ACQUIRE, "wavefront");
}

DI float opq(float v) { asm("" : "+v"(v)); return v; }
DI void cmul(f32x16& xr, f32x16& xi, int i, float c, float s) {
    const float a = xr[i], b = xi[i];
    xr[i] = opq(fmaf(a, c, -opq(b * s))); xi[i] = opq(fmaf(a, s, opq(b * c)));
}
DI void cmul_conj(f32x16& xr, f32x16& xi, int i, float c, float s) {
    const float a = xr[i], b = xi[i];
    xr[i] = opq(fmaf(a, c, opq(b * s))); xi[i] = opq(fmaf(b, c, -opq(a * s)));
}
struct Cm { u32x4 c[2], s[2], n[2]; };
DI void cmul_ab(const u32x4 (&ar)[2], const u32x4 (&ai)[2], const u32x4 (&c)[2], const u32x4 (&s)[2], const u32x4 (&n)[2], f32x16& xr, f32x16& xi) {
    xr = f32x16{}; xi = f32x16{};
#pragma unroll
    for (int k = 0; k < 2; ++k) {
        xr = mfma32(ar[k], c[k], xr); xi = mfma32(ar[k], s[k], xi);
        xr = mfma32(ai[k], n[k], xr); xi = mfma32(ai[k], c[k], xi);
    }
}
DI void cmul_ba(const u32x4 (&br)[2], const u32x4 (&bi)[2], const u32x4 (&c)[2], const u32x4 (&s)[2], const u32x4 (&n)[2], f32x16& xr, f32x16& xi) {
    xr = f32x16{}; xi = f32x16{};
#pragma unroll
    for (int k = 0; k < 2; ++k) {
        xr = mfma32(c[k], br[k], xr); xi = mfma32(s[k], br[k], xi);
        xr = mfma32(n[k], bi[k], xr); xi = mfma32(c[k], bi[k], xi);
    }
}
struct Comb { float kd, ph, sg; };
DI Comb comb(int k1, int ri_t, int slot, int ri_c) {
    int kd = k1 + 128 * slot;
    float ph = ri_t == ri_c ? 0.f : (ri_c ? 0.25f : 0.75f), sg = (slot >= 4 && ri_c) ? -1.f : 1.f;
    if (k1 == 0) {
        kd = slot < 4 ? 64 + 128 * slot : 128 * (slot - 3);
        ph = ri_c ? 0.25f : 0.f;
        if (slot == 7) { kd = ri_c ? 512 : 0; ph = 0.f; }
        sg = (ri_t == (slot < 4 ? 1 : 0)) ? 1.f : 0.f;
    }
    return Comb{(float)kd * (1.f / 1024), ph, sg};
}
DI float comb_at(const Comb& c, float dh) { return c.sg * __builtin_amdgcn_cosf(fmaf(c.kd, dh, c.ph)); }
constexpr int FFT_ROUNDS = 3;
constexpr unsigned P2_ZERO = 131072, P2_DUMP = P2_ZERO + 2048, P2_MAT = P2_DUMP + 2048, LDS_WORD = P2_MAT + 8192;

DI void spec_setup(const int pw, char* smem) {
    const int tid = threadIdx.x, lane = tid & 63, wv = __builtin_amdgcn_readfirstlane(tid >> 6);
    const int l31 = lane & 31, h = lane >> 5, r1 = wv >> 2;
    if (tid < 128) *(u32x4*)(smem + P2_ZERO + tid * 16) = u32x4{0, 0, 0, 0};
    {
        const float fb = (float)l31 * (1.f / 32), eb = r1 ? (float)(2 * l31 + 1) * (1.f / 128) : fb;
        const float km = r1 ? 2.f : 1.f, c0 = (float)(8 * (wv & 3) + 4 * h), k0 = r1 ? fmaf(2.f, c0, 1.f) : c0;
        float fc[4], fs[4], ec[4], es[4];
        u32x4 tr, ti;
#pragma unroll
        for (int e = 0; e < 4; ++e) {
            const float fa = fb * (c0 + (float)e), ea = eb * fmaf(km, (float)e, k0);
            fc[e] = __builtin_amdgcn_cosf(fa); fs[e] = __builtin_amdgcn_sinf(fa);
            ec[e] = __builtin_amdgcn_cosf(ea); es[e] = __builtin_amdgcn_sinf(ea);
            tr[e] = __float_as_uint(__builtin_amdgcn_cosf(ea * (1.f / 32)));
            ti[e] = __float_as_uint(-__builtin_amdgcn_sinf(ea * (1.f / 32)));
        }
        u32x4* cp = (u32x4*)(smem + wv * 4096) + lane;
        cp[0] = u32x4{pk(fc[0], fc[1]), pk(fc[2], fc[3]), pk(fs[0], fs[1]), pk(fs[2], fs[3])};
        cp[64] = u32x4{pk(ec[0], ec[1]), pk(ec[2], ec[3]), pk(es[0], es[1]), pk(es[2], es[3])};
        cp[128] = tr; cp[192] = ti;
    }
    const int k1 = 2 * pw + (wv & 1), ks = (wv >> 1) & 1;
    const unsigned neg = (l31 & 2) ? 0x80008000u : 0u;
    u32x4 m;
    if (wv < 4) {
        const Comb cb = comb(k1, h, l31 >> 2, l31 & 1);
#pragma unroll
        for (int e = 0; e < 4; ++e) m[e] = pk(comb_at(cb, (float)(2 * e)), comb_at(cb, (float)(2 * e + 1))) ^ (ks ? neg : 0u);
    } else {
#pragma unroll
        for (int u = 0; u < 2; ++u) {
            const int slot = 4 * ks + 2 * h + u;
            const float sc = (k1 == 0 && slot < 7) ? 0x1p-20f : 0x1p-21f, dh = (float)(l31 >> 2);
            m[2 * u] = pk(sc * comb_at(comb(k1, l31 & 1, slot, 0), dh), sc * comb_at(comb(k1, l31 & 1, slot, 1), dh));
            m[2 * u + 1] = m[2 * u] ^ neg;
        }
    }
    *(u32x4*)(smem + P2_MAT + wv * 1024 + lane * 16) = m;
}

DI void spectral(const int pw, char* __restrict__ ws, char* smem, __amdgpu_buffer_rsrc_t srdP, u64 tok) {
    const int tid = threadIdx.x, lane = tid & 63, wv = __builtin_amdgcn_readfirstlane(tid >> 6);
    const int l31 = lane & 31, h = lane >> 5, r1 = wv >> 2;
    const unsigned lds = (unsigned)(size_t)smem;
    const int nch = (pw == 0 && (wv == 3 || wv == 6)) ? 5 : 4;
    auto filter_ptr = [&](int c) {
        const int slot = 2 * (wv & 3) + (c & 1), k1 = 2 * pw + ((c >> 1) & 1);
        int col = slot < 4 ? k1 + 128 * slot : 1024 - k1 - 128 * slot;
        if (k1 == 0) col = slot < 4 ? 64 + 128 * slot : slot < 7 ? 128 * (slot - 3) : 0;
        if (c >= 4) col = 512;
        return (unsigned)WS_W + (col * 2 + r1) * 4096 + lane * 16;
    };
    Cm E, Fm;
    float twr[16], twi[16];
    {
        lds_bar();
#pragma unroll
        for (int u = 0; u < 4; ++u) {
            const u32x4* rp = (const u32x4*)(smem + (4 * r1 + u) * 4096) + lane;
            const u32x4 f = rp[0], e = rp[64], a = rp[128], c = rp[192];
#pragma unroll
            for (int k = 0; k < 2; ++k) {
                Fm.c[u >> 1][2 * (u & 1) + k] = f[k]; Fm.n[u >> 1][2 * (u & 1) + k] = f[2 + k]; Fm.s[u >> 1][2 * (u & 1) + k] = f[2 + k] ^ 0x80008000u;
                E.c[u >> 1][2 * (u & 1) + k] = e[k]; E.n[u >> 1][2 * (u & 1) + k] = e[2 + k]; E.s[u >> 1][2 * (u & 1) + k] = e[2 + k] ^ 0x80008000u;
            }
#pragma unroll
            for (int k = 0; k < 4; ++k) { twr[4 * u + k] = __uint_as_float(a[k]); twi[4 * u + k] = __uint_as_float(c[k]); }
        }
    }
    auto mat = [&](int i) { return *(const u32x4*)(smem + P2_MAT + i * 1024 + lane * 16); };
    if (wv < 4) wait_flag((u64*)(ws + WS_FLAG + FL_TW + tid * 32), tok);
    lds_bar();
    if (pw == 0 && wv == 0) set_flag((u64*)(ws + WS_FLAG + FL_EPOCH + lane * 8), tok);
    u32x4 ta[2][4][2], wn[4];
    const auto srdS = srd(ws, WS_FLAG);
    const unsigned tp = WS_T + pw * 131072 + (4 * wv) * 1024 + l31 * 32 + h * 16;
    auto load_t = [&](int u) {
#pragma unroll
        for (int k = 0; k < 4; ++k) ta[u >> 1][2 * (u & 1) + (k >> 1)][k & 1] = ld_dev(srdS, tp + (u >> 1) * 65536 + (2 * (u & 1) + (k >> 1)) * 1024 + (k & 1) * 32768);
        asm volatile("" ::: "memory");
    };
    load_t(0);
    load_t(1);
#pragma unroll
    for (int gi = 0; gi < 2; ++gi) {
#pragma unroll
        for (int t = 0; t < 4; ++t) {
            if (gi == 0 && t == 1) load_t(2);
            if (gi == 0 && t == 3) load_t(3);
            if (gi == 1 && t == 1) {
                const unsigned wp = filter_ptr(0);
#pragma unroll
                for (int cc = 0; cc < 4; ++cc) wn[cc] = ld_dev(srdS, wp + cc * 1024);
            }
            asm volatile("" : "+v"(ta[gi][t][0]), "+v"(ta[gi][t][1])::"memory");
            f32x16 d = mfma32(ta[gi][t][0], mat(gi), f32x16{});
            d = mfma32(ta[gi][t][1], mat(2 + gi), d);
            char* p = smem + gi * 65536 + l31 * 2048 + h * 512 + (((4 * wv + t) ^ (l31 & 7)) << 4);
            *(u32x4*)p = pack8(d, 0);
            *(u32x4*)(p + 1024) = pack8(d, 1);
        }
    }
    __syncthreads();
    unsigned sink = 0;
#pragma unroll 1
    for (int c = 0; c < nch; ++c) {
        const int q2 = c * 256 + pw * 8 + wv;
        touch(sink, srdP, c < FFT_ROUNDS ? ((q2 & 1 ? 1792 : 256) + (q2 >> 1) * 2) * (S * 2) + lane * 128 : 0x80000000u);
        if ((c ^ r1) & 1) __builtin_amdgcn_s_setprio(1); else __builtin_amdgcn_s_setprio(0);
        u32x4 w[4];
#pragma unroll
        for (int cc = 0; cc < 4; ++cc) w[cc] = wn[cc];
        const bool real = pw == 0 && (c == 4 || (c == 1 && (wv & 3) == 3));
        const int rre = c == 4 ? 29 + 2 * r1 : 8 * (wv & 3) + 4 * (c & 1) + 2 * r1, rim = rre + 1;
        char* bre = smem + ((c >> 1) & 1) * 65536 + rre * 2048;
        u32x4 ar[2], ai[2];
        {
            const char* pr = bre + h * 512 + ((l31 ^ (rre & 7)) << 4);
            const char* pi = (real ? smem + P2_ZERO : bre + 2048) + h * 512 + ((l31 ^ (rim & 7)) << 4);
            ar[0] = *(const u32x4*)pr; ar[1] = *(const u32x4*)(pr + 1024);
            ai[0] = *(const u32x4*)pi; ai[1] = *(const u32x4*)(pi + 1024);
        }
        {
            const unsigned wp = filter_ptr(c + 1);
#pragma unroll
            for (int cc = 0; cc < 4; ++cc) wn[cc] = ld_dev(srdS, wp + cc * 1024);
        }
        f32x16 xr, xi;
        cmul_ab(ar, ai, E.c, E.s, E.n, xr, xi);
#pragma unroll
        for (int i = 0; i < 16; ++i) cmul(xr, xi, i, twr[i], twi[i]);
        ar[0] = pack8(xr, 0); ar[1] = pack8(xr, 1); ai[0] = pack8(xi, 0); ai[1] = pack8(xi, 1);
        cmul_ba(ar, ai, Fm.c, Fm.s, Fm.n, xr, xi);
#pragma unroll
        for (int i = 0; i < 16; ++i) cmul(xr, xi, i, lo16(w[i >> 2][i & 3]), hi16(w[i >> 2][i & 3]));
        ar[0] = pack8(xr, 0); ar[1] = pack8(xr, 1); ai[0] = pack8(xi, 0); ai[1] = pack8(xi, 1);
        cmul_ab(ar, ai, Fm.c, Fm.n, Fm.s, xr, xi);
#pragma unroll
        for (int i = 0; i < 16; ++i) cmul_conj(xr, xi, i, twr[i], twi[i]);
        ar[0] = pack8(xr, 0); ar[1] = pack8(xr, 1); ai[0] = pack8(xi, 0); ai[1] = pack8(xi, 1);
        cmul_ab(ar, ai, E.c, E.n, E.s, xr, xi);
        const int sw = ((l31 & 3) * 8 + h) ^ ((l31 >> 1) & 7);
        char* qr = bre + (l31 >> 2) * 256;
        char* qi = (real ? smem + P2_DUMP : bre + 2048) + (l31 >> 2) * 256;
        const int kr = sw ^ (rre & 1) ^ ((rre & 2) << 3), ki = sw ^ (rim & 1) ^ ((rim & 2) << 3);
#pragma unroll
        for (int gq = 0; gq < 4; ++gq) {
            *(u32x2*)(qr + ((kr ^ (2 * gq)) << 3)) = u32x2{pk(xr[4 * gq], xr[4 * gq + 1]), pk(xr[4 * gq + 2], xr[4 * gq + 3])};
            *(u32x2*)(qi + ((ki ^ (2 * gq)) << 3)) = u32x2{pk(xi[4 * gq], xi[4 * gq + 1]), pk(xi[4 * gq + 2], xi[4 * gq + 3])};
        }
    }
    __builtin_amdgcn_s_setprio(0);
    asm volatile("s_waitcnt vmcnt(0)" : "+v"(sink)::"memory");
    __syncthreads();
    const int blk = (lane >> 4) & 1, qq = (lane >> 2) & 3, p = lane & 3;
    const auto srdT = srd(ws + WS_TP, 4u << 20);
    const unsigned op = ((wv * 8 + h) * 8 + (pw >> 2)) * 4096 + blk * 1024 + (16 * (pw & 3) + (lane & 15)) * 16;
#pragma unroll
    for (int a = 0; a < 2; ++a) {
        f32x16 d[2][2];
#pragma unroll
        for (int bb = 0; bb < 2; ++bb) {
            const int jp = 4 * wv + 2 * a + blk;
            const int pos = ((jp & 3) * 8 + 2 * p + ((bb ^ p) & 1)) ^ ((qq & 1) | ((qq & 2) << 3)) ^ ((jp >> 1) & 7);
#pragma unroll
            for (int gi = 0; gi < 2; ++gi) {
                const unsigned ad = lds + gi * 65536 + (8 * h + qq) * 2048 + wv * 256 + pos * 8;
                d[gi][bb] = mfma32(mat(4 + gi), cat(trread(ad), trread(ad + 8192)), f32x16{});
                d[gi][bb] = mfma32(mat(6 + gi), cat(trread(ad + 32768), trread(ad + 32768 + 8192)), d[gi][bb]);
            }
        }
#pragma unroll
        for (int c4 = 0; c4 < 4; ++c4)
#pragma unroll
            for (int j1 = 0; j1 < 2; ++j1) {
                const int r = 4 * c4 + 2 * j1;
                st_dev(u32x4{pk(d[0][0][r], d[0][0][r + 1]), pk(d[1][0][r], d[1][0][r + 1]), pk(d[0][1][r], d[0][1][r + 1]), pk(d[1][1][r], d[1][1][r + 1])},
                       srdT, op + (j1 * 64 + 2 * c4) * 32768 + a * 2048);
            }
    }
}

template <int CTRL> DI float dpp_add(float v) {
    return v + __int_as_float(__builtin_amdgcn_update_dpp(0, __float_as_int(v), CTRL, 0xf, 0xf, true));
}
DI void sync4(unsigned addr) {
    unsigned seen;
    asm volatile("s_waitcnt lgkmcnt(0)\n s_mov_b64 exec, 1\n ds_add_u32 %1, %2\n"
                 "Lw%=:\n ds_read_b32 %0, %1\n s_waitcnt lgkmcnt(0)\n v_cmp_gt_u32 vcc, 4, %0\n s_cbranch_vccnz Lw%=\n s_mov_b64 exec, -1"
                 : "=&v"(seen) : "v"(addr), "v"(1u) : "memory", "vcc");
}
DI f32x2 row_stats(const u32x4 (&v)[4], float* red, unsigned word, int wave, int lane) {
    f32x4 b;
#pragma unroll
    for (int k = 0; k < 4; ++k) {
        float s = 0.f, q = 0.f;
#pragma unroll
        for (int e = 0; e < 4; ++e) {
            const unsigned w = v[k][e];
            const bf16x2 p = __builtin_bit_cast(bf16x2, w);
            s = __builtin_amdgcn_fdot2_f32_bf16(p, __builtin_bit_cast(bf16x2, 0x3f803f80u), s, false);
            q = __builtin_amdgcn_fdot2_f32_bf16(p, p, q, false);
        }
        auto r = __builtin_amdgcn_permlane16_swap(__float_as_uint(s), __float_as_uint(q), false, false);
        b[k] = dpp_add<0x121>(dpp_add<0x122>(dpp_add<0x124>(dpp_add<0x128>(__uint_as_float(r[0]) + __uint_as_float(r[1])))));
    }
    if ((lane & 15) == 0) *(f32x4*)(red + wave * 16 + ((lane >> 4) & 1) * 8 + (lane >> 5) * 4) = b;
    sync4(word);
    const float* rp = red + (lane >> 5) * 4 + (lane & 3);
    const float m = ((rp[0] + rp[16]) + (rp[32] + rp[48])) * (1.f / D);
    const float q = ((rp[8] + rp[24]) + (rp[40] + rp[56])) * (1.f / D);
    const float ri = __builtin_amdgcn_rcpf(rbf(__builtin_amdgcn_sqrtf(rbf(rbf(fmaxf(q - m * m, 0.f)) + 1e-5f))));
    return f32x2{ri, -rbf(m) * ri};
}
template <int R> DI f32x2 quad_bcast(f32x2 v) {
    return f32x2{__int_as_float(__builtin_amdgcn_update_dpp(0, __float_as_int(v[0]), R * 0x55, 0xf, 0xf, true)),
                 __int_as_float(__builtin_amdgcn_update_dpp(0, __float_as_int(v[1]), R * 0x55, 0xf, 0xf, true))};
}

constexpr unsigned CODE_BYTES = 37376;
constexpr int XP = 2112;
constexpr int WSEG = 144;

DI void arrive(unsigned addr, unsigned n, u64* f, u64 tok) {
    if ((threadIdx.x & 63) == 0 && __hip_atomic_fetch_add((LDS unsigned*)(size_t)addr, 1u, __ATOMIC_RELAXED, __HIP_MEMORY_SCOPE_WORKGROUP) == n - 1) set_flag(f, tok);
}

DI void phase0(const unsigned short* __restrict__ x, const unsigned short* __restrict__ wr, const unsigned short* __restrict__ wi,
               const unsigned short* __restrict__ w1, const unsigned short* __restrict__ b1, const unsigned short* __restrict__ b2,
               char* __restrict__ ws, char* smem0, u64& tok) {
    char* smem = smem0 + 65536;
    const int b = blockIdx.x, tid = threadIdx.x, lane = tid & 63, wave = __builtin_amdgcn_readfirstlane(tid >> 6);
    const auto srdS = srd(ws, WS_FLAG);
    const unsigned words = (unsigned)(size_t)smem + 8 * XP + 512 + 128 * WSEG;
    const int s0 = b & 31, jb = (b >> 5) & 3, j1 = b >> 7, h = lane >> 5, l31 = lane & 31;
    u32x4 raw[4];
    if (wave < 4) {
        const unsigned short* rp = x + (size_t)(s0 + 256 * jb + 1024 * j1 + 128 * h) * D + 256 * wave + 8 * l31;
#pragma unroll
        for (int r = 0; r < 4; ++r) raw[r] = *(const u32x4*)(rp + (size_t)r * 32 * D);
    }
    if (tid >= 448 && tid < 457) *(unsigned*)(smem + 8 * XP + 512 + 128 * WSEG + (tid - 448) * 4) = 0u;
    lds_bar();
    auto token = [&]() {
        while (__hip_atomic_load((LDS unsigned*)(size_t)(words + 20), __ATOMIC_ACQUIRE, __HIP_MEMORY_SCOPE_WAVEFRONT) == 0u) {}
        const u32x2 t = *(const volatile LDS u32x2*)(size_t)(words + 24);
        return ((u64)__builtin_amdgcn_readfirstlane(t[1]) << 32) | (unsigned)__builtin_amdgcn_readfirstlane(t[0]);
    };
    u64* fl_tw = (u64*)(ws + WS_FLAG + FL_TW + b * 32);
    if (wave >= 4) {
        const int t2 = tid - 256, rb = b & 3, qb = (b >> 2) & 7, cr = b >> 5;
        char* img = smem + 8 * XP + 512;
        u64 epoch = 0;
        if (wave == 4) asm volatile("global_load_dwordx2 %0, %1, off sc1" : "=v"(epoch) : "v"(ws + WS_FLAG + FL_EPOCH + lane * 8) : "memory");
        const char* code;
        asm volatile("s_getpc_b64 s[40:41]\n s_add_u32 s40, s40, k_all@rel32@lo+4\n s_addc_u32 s41, s41, k_all@rel32@hi+12" : "={s[40:41]}"(code)::"scc");
        unsigned sink = 0;
        touch(sink, srd(code, b < 16 ? CODE_BYTES : 0), ((b >> 3) * 256 + t2) * 128u);
#pragma unroll
        for (int k = 0; k < 5; ++k) {
            if (k == 4 && wave >= 6) break;
            const int c = k * 256 + t2, seg = (c * 7282) >> 16, ch = c - seg * 9;
            const int row = 16 * rb + (seg & 15) + 64 * (4 * qb + ((seg >> 4) & 3));
            glds((const char*)(seg < 64 ? wr : wi) + ((row * 1026 + cr * 128) & ~15) + ch * 16, (unsigned)(size_t)img + k * 4096 + (wave - 4) * 1024);
        }
        sync4(words + 32);
        touch(sink, srd(w1, b < 128 ? 0 : 512 * F * 2), (unsigned)((b - 128) * 4 + wave - 4) * (F * 2) + min(lane, 55) * 128u);
        touch(sink, srd(wave & 1 ? b1 : b2, b != 16 ? 0 : wave & 1 ? F * 2 : S * 2), lane * 128u);
        if (wave == 4) {
            asm volatile("s_waitcnt vmcnt(8)" : "+v"(epoch)::"memory");
            asm volatile("ds_add_u64 %0, %1\n s_waitcnt lgkmcnt(0)" ::"v"(words + 24), "v"(lane ? epoch : 2 * epoch + 0x9e3779b97f4a7c15ull) : "memory");
            if (lane == 0) *(volatile LDS unsigned*)(size_t)(words + 20) = 1u;
        }
        if (b < 32) spec_setup(b, smem0);
        asm volatile("s_waitcnt vmcnt(2)" ::: "memory");
        sync4(words + 8);
        const int rr = t2 & 7, r1 = (t2 >> 3) & 1, cl = t2 >> 4, k16 = 2 * rr + r1;
        const char* lp = img + k16 * WSEG + 2 * (k16 & 7) + 2 * cl;
        const unsigned dst = WS_W + ((64 * cr + cl) * 2 + r1) * 4096 + qb * 512 + rb * 128 + rr * 16;
#pragma unroll
        for (int ps = 0; ps < 5; ++ps) {
            if (ps == 4 && (cr != 7 || cl != 0)) break;
            u32x4 o;
#pragma unroll
            for (int e = 0; e < 4; ++e)
                o[e] = (unsigned)*(const unsigned short*)(lp + e * 16 * WSEG + ps * 32) | ((unsigned)*(const unsigned short*)(lp + (64 + e * 16) * WSEG + ps * 32) << 16);
            st_dev(o, srdS, dst + ps * 16 * 8192);
        }
        asm volatile("s_waitcnt vmcnt(0)" : "+v"(sink)::"memory");
        tok = token();
        arrive(words + 16, 8, fl_tw, tok);
        return;
    }
    u32x4 fm[4];
    const int n = 32 * wave + l31;
    {
        const float mul = (float)(n == 1 ? 64 : n >> 1) * (1.f / 128), a0 = ((n & 1) && n > 1 ? 0.25f : 0.f) + mul * (float)(8 * h);
#pragma unroll
        for (int ks = 0; ks < 4; ++ks) {
#pragma unroll
            for (int e = 0; e < 4; ++e)
                fm[ks][e] = pk(__builtin_amdgcn_cosf(fmaf(mul, (float)(16 * ks + 2 * e), a0)), __builtin_amdgcn_cosf(fmaf(mul, (float)(16 * ks + 2 * e + 1), a0)));
            asm volatile("" : "+v"(fm[ks]));
        }
    }
    if (b < 32) spec_setup(b, smem0);
    asm volatile("" : "+v"(raw[0]), "+v"(raw[1]), "+v"(raw[2]), "+v"(raw[3]));
    float* red = (float*)(smem + 8 * XP);
    const f32x2 st1 = row_stats(raw, red, words, wave, lane);
    const f32x2 n1[4] = {quad_bcast<0>(st1), quad_bcast<1>(st1), quad_bcast<2>(st1), quad_bcast<3>(st1)};
#pragma unroll
    for (int r = 0; r < 4; ++r) {
#pragma unroll
        for (int e = 0; e < 4; ++e) raw[r][e] = pk(fmaf(lo16(raw[r][e]), n1[r][0], n1[r][1]), fmaf(hi16(raw[r][e]), n1[r][0], n1[r][1]));
        *(u32x4*)(smem + (4 * h + r) * XP + 512 * wave + 16 * l31) = raw[r];
    }
    const f32x2 st2 = row_stats(raw, red + 64, words + 4, wave, lane);
    {
        const int blk = (lane >> 4) & 1, qq = (lane >> 2) & 3, p = lane & 3;
        const unsigned base = (unsigned)(size_t)smem + (2 * blk + (p & 1)) * XP + 16 * (8 * h + qq) + 8 * (p >> 1);
        const unsigned sg = (n & 2) ? 0x80008000u : 0u;
        const unsigned tp = WS_T + (n >> 2) * 131072 + ((j1 * 32 + s0) * 32 + 8 * jb + h) * 32 + (n & 2) * 32 + (n & 1) * 16;
        f32x16 acc[2] = {};
#pragma unroll
        for (int ks = 0; ks < 8; ++ks) {
            const u32x4 f = ks < 4 ? fm[ks & 3] : fm[ks & 3] ^ sg;
#pragma unroll
            for (int mt = 0; mt < 2; ++mt) {
                const unsigned a = base + mt * 4 * XP + ks * 256;
                acc[mt] = mfma32(cat(trread(a), trread(a + 64)), f, acc[mt]);
            }
        }
        const f32x2 n2[4] = {quad_bcast<0>(st2), quad_bcast<1>(st2), quad_bcast<2>(st2), quad_bcast<3>(st2)};
        auto hval = [&](int r, int c) { return fmaf(c & 1 ? hi16(raw[r][c >> 1]) : lo16(raw[r][c >> 1]), n2[r][0], n2[r][1]); };
        const unsigned hp = WS_H + (((2 * wave + (l31 >> 4)) * 64 + 32 * j1 + 8 * jb + (s0 >> 2)) * 8 + 4 * h) * 1024 + (16 * (s0 & 3) + (l31 & 15)) * 16;
#pragma unroll
        for (int e = 0; e < 4; ++e) {
            u32x4 o;
#pragma unroll
            for (int k = 0; k < 2; ++k) {
                const unsigned lo = pk(hval(2 * k, e), hval(2 * k + 1, e)), hi = pk(hval(2 * k, e + 4), hval(2 * k + 1, e + 4));
                auto r = __builtin_amdgcn_permlane32_swap(lo, hi, false, false);
                o[k] = r[0]; o[2 + k] = r[1];
            }
            st_dev(o, srdS, hp + e * 1024);
        }
        u32x4 tq[4];
#pragma unroll
        for (int mt = 0; mt < 2; ++mt) {
            const u32x4 lo = pack8(acc[mt], 0), hi = pack8(acc[mt], 1);
            u32x4 even, odd;
#pragma unroll
            for (int e = 0; e < 4; ++e) {
                const unsigned got = __builtin_amdgcn_update_dpp(0, (n & 2) ? lo[e] : hi[e], 0x4e, 0xf, 0xf, true);
                even[e] = (n & 2) ? got : lo[e]; odd[e] = (n & 2) ? hi[e] : got;
            }
            tq[2 * mt] = even; tq[2 * mt + 1] = odd;
        }
        tok = token();
        asm volatile("s_waitcnt vmcnt(0)" : "+v"(tq[0]), "+v"(tq[1]), "+v"(tq[2]), "+v"(tq[3])::"memory");
        arrive(words + 12, 4, (u64*)(ws + WS_FLAG + FL_H + b * 32), tok);
#pragma unroll
        for (int u = 0; u < 4; ++u) st_dev(tq[u], srdS, tp + (u >> 1) * 128 + (u & 1) * 65536);
    }
    asm volatile("s_waitcnt vmcnt(0)" ::: "memory");
    arrive(words + 16, 8, fl_tw, tok);
}

constexpr int SMEM_MAIN = LDS_WORD + 64;
constexpr int EXT_A = 7, EXT_B = 15, EXT_C = 23, X_SLEEP = 40;
constexpr int MID_BARRIERS = 32, OUT_BARRIERS = 28;

extern "C" __global__ __launch_bounds__(512, 2) void k_all(const unsigned short* __restrict__ x, const unsigned short* __restrict__ wr,
                                                           const unsigned short* __restrict__ wi, const unsigned short* __restrict__ w1,
                                                           const unsigned short* __restrict__ b1, const unsigned short* __restrict__ w2,
                                                           const unsigned short* __restrict__ b2, unsigned short* __restrict__ out, char* __restrict__ ws) {
    extern __shared__ __attribute__((aligned(16))) char smem[];
    const int b = blockIdx.x, xc = b & 7, idx = b >> 3;
    const int wave = __builtin_amdgcn_readfirstlane(threadIdx.x >> 6);
    const unsigned lds = (unsigned)(size_t)smem, ldsw = lds + wave * 2048;
    auto flag = [&](unsigned off) { return (u64*)(ws + WS_FLAG + off); };
    const int sel = (0x76532410u >> (4 * (idx >> 2))) & 7;
    const int kh = xc & 1, st = 4 * (xc >> 1) + (sel >> 1), dt = 4 * (sel & 1) + (idx & 3);
    const auto srdG = srd(ws + WS_G, D * F * 2), srdW = srd(w2, S * F * 2);
    const int rot = 2 - 2 * kh;
    const unsigned sW0 = st * 256 + (kh * 1792 + rot * 128) * (S * 2);
    const unsigned incW = 32 * S * 2;
    auto w2_head = [&](bool second) {
        const u32x2 vW = gemm_wsrc<false>(wave, threadIdx.x & 63, S * 2);
        unsigned sW = sW0 + (second ? W2_FIRST * incW : 0);
        if (second) asm volatile(GEMM2_PROW2 : [sW] "+s"(sW) : "{v[208:209]}"(vW), [srdW] "s"(srdW), [incW] "s"(incW), [ldsw] "s"(ldsw) : "memory", "scc", "m0");
        else asm volatile(GEMM2_PROW : [sW] "+s"(sW) : "{v[208:209]}"(vW), [srdW] "s"(srdW), [incW] "s"(incW), [ldsw] "s"(ldsw) : "memory", "scc", "m0");
    };
    if (threadIdx.x == 384) *(unsigned*)(smem + LDS_WORD) = 0u;
    u64 tok;
    phase0(x, wr, wi, w1, b1, b2, ws, smem, tok);
    if (idx < 4) {
        spectral(idx * 8 + xc, ws, smem, srdW, tok);
        lds_bar();
        if (wave < 4) { w2_head(false); w2_head(true); }
    } else if (wave >= 4) {
        bar(); bar(); bar(); bar();
        u64* hf = flag(FL_H + (threadIdx.x - 256) * 32);
        const u64 got = peek_flag(hf);
        bar(); bar();
        if (!__all(got == tok)) wait_flag(hf, tok);
#pragma unroll 1
        for (int k = 5; k < MID_BARRIERS + 1; ++k) bar();
    } else {
        int lane = threadIdx.x & 63;
        const int t = idx - 4, d1 = 4 * (xc & 1) + (t & 3);
        const unsigned ford = xc & 4 ? (xc & 2 ? 0x6532140u : 0x6542103u) : (xc & 2 ? 0x5431062u : 0x6432051u);
        const int ft = 7 * (xc >> 1) + ((ford >> (4 * (t >> 2))) & 7);
        unsigned sH = d1 * (64 * 8192) + wave * 2048, sW1 = ft * 256, cnt;
        const unsigned vH = lane * 16, g = lane >> 4;
        const u32x2 vW = gemm_wsrc<true>(wave, lane, F * 2);
        const auto srdH = srd(ws + WS_H, 4u << 20), srdW1 = srd(w1, S * F * 2);
        u32x32 h0, h1, bt;
        asm volatile(GEMM1_PROW
                     : [sW] "+s"(sW1), "=&{v[112:143]}"(bt)
                     : "{v[208:209]}"(vW), [srdW] "s"(srdW1), [incW] "s"(4 * F * 2), [ldsw] "s"(ldsw),
                       [vB0] "v"(16 * g + 8 * (g & 1)), [vB1] "v"(16 * g + 8 - 8 * (g & 1)), [srdB] "s"(srd(b1, F * 2)), [sB] "s"(ft * 256)
                     : "memory", "scc", "m0");
        if (wave == 0) wait_flag(flag(FL_H + lane * 32), tok);
        bar();
        unsigned sink;
        asm volatile(GEMM1_PRO
                     : [sH] "+s"(sH), "=&{v[144:175]}"(h0), "=&{v[176:207]}"(h1), "+{v[112:143]}"(bt), "=&{v234}"(sink), "+v"(lane)
                     : [vH] "v"(vH), [srdH] "s"(srdH), [vN] "v"(0x80000000u)
                     : "memory", "scc");
        u32x16 vR;
#pragma unroll
        for (int e = 0; e < 16; ++e) vR[e] = gemm_rdaddr<true>(lds, lane, e >> 1, 0, e & 1);
        const int gam = ((t & 3) + (t >> 2)) & 3;
        const unsigned vT = (256 * (gam + 2) + 128 * (xc & 1) + 32 * wave + (lane >> 1)) * (F * 2) + ft * 256 + (lane & 1) * 128, vT2 = gam >= 2 ? vT + 512 * F * 2 : 0x80000000u;
        f32x16 a[4];
        asm volatile(GEMM1_ASM
                     : [sH] "+s"(sH), [sW] "+s"(sW1), [cnt] "=&s"(cnt), "+{v[144:175]}"(h0), "+{v[176:207]}"(h1),
                       "+{v[112:143]}"(bt), "=&{v[16:31]}"(a[0]), "=&{v[32:47]}"(a[1]), "=&{v[48:63]}"(a[2]), "=&{v[64:79]}"(a[3]), "+{v234}"(sink)
                     : [vH] "v"(vH), "{v[208:209]}"(vW), "{v[210:225]}"(vR), [srdH] "s"(srdH), [srdW] "s"(srdW1), [incW] "s"(4 * F * 2), [ldsw] "s"(ldsw),
                       [jmp] "s"((256 - 28) * F * 2), [vT] "v"(vT), [vT2] "v"(vT2), [tcnt] "s"(7 - gam)
                     : "memory", "scc", "m0", GEMM1_CLOB);
        bar();
        w2_head(false);
        const unsigned gp = ((d1 * 112 + 4 * ft) * 8 + 2 * wave) * 1024 + lane * 16;
#pragma unroll
        for (int q = 0; q < 8; ++q) {
            const f32x16& k = a[q >> 1];
            const int o = 8 * (q & 1);
            u32x4 r;
#pragma unroll
            for (int u = 0; u < 4; ++u) r[u] = gelu2(k[o + 2 * u], k[o + 2 * u + 1]);
            st_dev(r, srdG, gp + (q & 3) * 8192 + (q >> 2) * 1024);
        }
        asm volatile("s_waitcnt vmcnt(0)" : "+{v234}"(sink)::"memory");
        if (lane == 0) set_flag(flag(FL_G + ((d1 * 28 + ft) * 4 + wave) * 128), tok);
        w2_head(true);
    }
    int lane = threadIdx.x & 63;
    asm volatile("" : "+v"(lane));
    if (wave >= 4) {
        const int hw = wave - 4, i = lane & 15, g = lane >> 4, tid = threadIdx.x - 256;
        u32x2 G[8];
        {
            const float dl = (float)(16 * dt + i), a1 = dl * (1.f / 128);
#pragma unroll
            for (int u = 0; u < 16; ++u) {
                const float ang = a1 * (float)(8 * (u >> 1) + 2 * g + (u & 1));
                G[u >> 1][u & 1] = pk(2.f * __builtin_amdgcn_cosf(ang), 2.f * __builtin_amdgcn_cosf(ang + 0.25f));
            }
            if (g == 0) G[0][0] = pk(1.f, (i & 1) ? -1.f : 1.f);
        }
        if (idx >= 4) __builtin_amdgcn_s_sleep(X_SLEEP);
        asm volatile("" ::: "memory");
        const unsigned o0 = (128 * st + 64 * kh + (tid >> 4)) * (D * 2) + (128 * dt + 8 * i) * 2;
        u32x4 xv[4];
        float bv[4];
#pragma unroll
        for (int it = 0; it < 4; ++it) {
            xv[it] = *(const u32x4*)((const char*)x + o0 + it * 16 * D * 2);
            bv[it] = lo16(b2[128 * st + 64 * kh + (tid >> 4) + 16 * it]);
        }
        asm volatile("s_waitcnt vmcnt(0)\n s_barrier" : "+v"(xv[0]), "+v"(xv[1]), "+v"(xv[2]), "+v"(xv[3]), "+v"(bv[0]), "+v"(bv[1]), "+v"(bv[2]), "+v"(bv[3])::"memory");
        if (idx < 4 && tid == 0) set_flag(flag(FL_TP + (idx * 8 + xc) * 128), tok);
        if (hw == 0) {
            u64* gf = flag(FL_G + ((dt * 28 + 14 * kh) * 4 + min(lane, 55)) * 128);
            const u64 got = peek_flag(gf);
            bar(); bar();
            if (!__all(got == tok)) wait_flag(gf, tok);
        } else { bar(); bar(); }
#pragma unroll 1
        for (int k = 3; k < OUT_BARRIERS - 6; ++k) bar();
        u64* fp = flag(FL_TP + (lane & 31) * 128);
        const u64 seen = peek_flag(fp);
        bar(); bar();
        if (!__all(seen == tok)) wait_flag(fp, tok);
        __builtin_amdgcn_fence(__ATOMIC_ACQUIRE, "wavefront");
        const auto srdT = srd(ws + WS_TP, 4u << 20);
        const unsigned tp = (st * 8 + 2 * hw) * 32768 + kh * 2048 + lane * 16;
        u32x4 tv[2][8][2];
#pragma unroll
        for (int u = 0; u < 32; ++u) {
            tv[u >> 4][(u >> 1) & 7][u & 1] = ld_dev(srdT, tp + (u >> 1) * 4096 + (u & 1) * 1024);
            if (u == EXT_A || u == EXT_B || u == EXT_C) bar();
        }
        bar(); bar();
        f32x4 c[2][4] = {};
#pragma unroll
        for (int m = 0; m < 2; ++m)
#pragma unroll
            for (int kap = 0; kap < 8; ++kap) {
                asm volatile("" : "+v"(tv[m][kap][0]), "+v"(tv[m][kap][1]));
#pragma unroll
                for (int e = 0; e < 4; ++e) {
                    const u32x4 t4 = tv[m][kap][e >> 1];
                    const u32x2 t2 = {t4[2 * (e & 1)], t4[2 * (e & 1) + 1]};
                    c[m][e] = __builtin_amdgcn_mfma_f32_16x16x16bf16_1k(__builtin_bit_cast(s16x4, t2), __builtin_bit_cast(s16x4, G[kap]), c[m][e], 0, 0, 0);
                }
            }
#pragma unroll
        for (int j = 0; j < 16; ++j) {
            const int row = 32 * (j >> 3) + 8 * g + 4 * (((j >> 2) ^ g) & 1) + (j & 3);
            *(f32x2*)(smem + 65536 + row * 512 + ((4 * i + (hw ^ (i >> 2) ^ (j & 1))) << 3)) = f32x2{c[0][j >> 2][j & 3], c[1][j >> 2][j & 3]};
        }
        sync4(lds + LDS_WORD);
        f32x2 z[4][4];
#pragma unroll
        for (int it = 0; it < 4; ++it)
#pragma unroll
            for (int w = 0; w < 4; ++w) {
                const int row = 16 * it + (tid >> 4), sw = (i >> 2) ^ (row & 1);
                z[it][w] = *(const f32x2*)(smem + 65536 + row * 512 + ((4 * i + (w ^ sw)) << 3)) + f32x2{lo16(xv[it][w]), hi16(xv[it][w])} + bv[it];
                asm volatile("" : "+v"(z[it][w]));
            }
        bar();
#pragma unroll
        for (int it = 0; it < 4; ++it) {
            const int row = 16 * it + (tid >> 4), sw = (i >> 2) ^ (row & 1);
            u32x4 o;
#pragma unroll
            for (int w = 0; w < 4; ++w) {
                const f32x2 y = *(const f32x2*)(smem + row * 512 + ((4 * i + (w ^ sw)) << 3)) + z[it][w];
                o[w] = pk(y[0], y[1]);
            }
            st_dev(o, srd(out, S * D * 2), o0 + it * 16 * D * 2);
        }
        return;
    }
    wait_flag(flag(FL_G + ((dt * 28 + 14 * kh + rot + min(lane, 2)) * 4 + wave) * 128), tok);
    asm volatile("s_waitcnt vmcnt(0)" ::: "memory");
    unsigned sH = (dt * 112 + 56 * kh + 4 * rot) * 8192 + wave * 2048, sW = sW0 + 6 * incW, cnt, sink, incX, incY;
    const unsigned vH = lane * 16;
    const u32x2 vW = gemm_wsrc<false>(wave, lane, S * 2);
    u32x32 h0, h1;
    asm volatile(GEMM2_PRO
                 : [sH] "+s"(sH), "=&{v[144:175]}"(h0), "=&{v[176:207]}"(h1), "=&{v234}"(sink), "+v"(lane)
                 : [vH] "v"(vH), [srdH] "s"(srdG), [vN] "v"(0x80000000u)
                 : "memory", "scc");
    const int i = lane & 15, g = lane >> 4;
    u32x16 vR;
#pragma unroll
    for (int e = 0; e < 16; ++e) vR[e] = gemm_rdaddr<false>(lds, lane, e >> 1, 2 * kh, (e ^ g) & 1);
    const int gam = (st + dt) & 3;
    const unsigned vT = (1792 * kh + (256 * (gam + 3) + 128 * rot) % 1792 + 128 * (dt >> 2) + 32 * wave + (lane >> 1)) * (S * 2) + st * 256 + (lane & 1) * 128;
    f32x16 a[4];
    asm volatile(GEMM2_ASM
                 : [sH] "+s"(sH), [sW] "+s"(sW), [cnt] "=&s"(cnt), [incX] "=&s"(incX), [incY] "=&s"(incY), "+{v[144:175]}"(h0), "+{v[176:207]}"(h1),
                   "=&{v[16:31]}"(a[0]), "=&{v[32:47]}"(a[1]), "=&{v[48:63]}"(a[2]), "=&{v[64:79]}"(a[3]), "+{v234}"(sink)
                 : [vH] "v"(vH), "{v[208:209]}"(vW), "{v[210:225]}"(vR), [srdH] "s"(srdG), [srdW] "s"(srdW), [incW] "s"(incW),
                   [ldsw] "s"(ldsw), [vT] "v"(vT), [tcnt] "s"(6 - gam), [jcnt] "s"(1 - kh), [jmp] "s"(-55 * incW), [jmpH] "s"(-55 * 8192)
                 : "memory", "scc", "m0", GEMM2_CLOB);
    const auto srdX = srd(ws + WS_X, 4u << 20);
    const unsigned xo = wave * 4096 + lane * 16;
#pragma unroll
    for (int q = 0; q < 4; ++q) st_dev(pack8(a[(q & 2) + 1], q & 1), srdX, (b ^ 1) * 16384 + xo + q * 1024);
    bar();
    asm volatile("s_waitcnt vmcnt(0)" ::: "memory");
    if (lane == 0) set_flag(flag(FL_X + ((b ^ 1) * 4 + wave) * 128), tok);
    wait_flag(flag(FL_X + (b * 4 + wave) * 128), tok);
    u32x4 p[4];
#pragma unroll
    for (int q = 0; q < 4; ++q) p[q] = ld_dev(srdX, b * 16384 + xo + q * 1024);
#pragma unroll
    for (int j = 0; j < 16; ++j) {
        const unsigned p0 = p[j >> 3][(j >> 1) & 3], p1 = p[2 + (j >> 3)][(j >> 1) & 3];
        const f32x2 y = {a[0][j] + (j & 1 ? hi16(p0) : lo16(p0)), a[2][j] + (j & 1 ? hi16(p1) : lo16(p1))};
        const int row = 32 * (j >> 3) + 8 * g + 4 * (((j >> 2) ^ g) & 1) + (j & 3);
        *(f32x2*)(smem + row * 512 + ((4 * i + (wave ^ (i >> 2) ^ (j & 1))) << 3)) = y;
    }
    lds_bar();
}

extern "C" void kernel_launch(void* const* d_in, const int* in_sizes, int n_in, void* d_out, int out_size, void* d_ws, size_t ws_size,
                              hipStream_t stream) {
    typedef const unsigned short* P;
    const P x = (P)d_in[0], wr = (P)d_in[1], wi = (P)d_in[2], w1 = (P)d_in[4], b1 = (P)d_in[5], w2 = (P)d_in[6], b2 = (P)d_in[7];
    char* ws = (char*)d_ws;
    (void)hipFuncSetAttribute((const void*)k_all, hipFuncAttributeMaxDynamicSharedMemorySize, SMEM_MAIN);
    k_all<<<256, 512, SMEM_MAIN, stream>>>(x, wr, wi, w1, b1, w2, b2, (unsigned short*)d_out, ws);
}
```
